```python
import torch

def _softmax(x, axis=-1):
    return torch.softmax(x, dim=axis)
def _gelu(x):
    return torch.nn.functional.gelu(x, approximate="tanh")
def _silu(x):
    return torch.nn.functional.silu(x)
def _sigmoid(x):
    return torch.sigmoid(x)
def _layernorm(x, axis=-1, eps=1e-5):
    m = x.mean(dim=axis, keepdim=True)
    v = x.var(dim=axis, keepdim=True, unbiased=False)
    return (x - m) / torch.sqrt(v + eps)
def _scatter_add(tgt, idx, src, axis=0):
    return tgt.clone().index_add_(axis, idx, src)
def _segment_sum(src, seg, n):
    out = torch.zeros((n,) + src.shape[1:], dtype=src.dtype)
    return out.index_add_(0, seg, src)
def _pad(x, pw):
    flat = tuple(v for pair in reversed(pw) for v in pair)
    return torch.nn.functional.pad(x, flat)

kernel_name = 'parallel_sum__sliding_block_cached__glu_gated_swiglu__decode'

def setup_inputs():

    _w = (lambda a: (lambda t: t.to(torch.bfloat16) if t.is_floating_point() else t)(torch.as_tensor(a)))
    def _A_setup():
        B, Sq, D = 1, 1, 640
        Hq, Hkv, Sk, Dh = 32, 32, 32768, 192
        assert Hq % Hkv == 0, (Hq, Hkv)
        return {
            "x": _w((lambda *s: torch.randn(*s, dtype=torch.bfloat16) * 0.05)(B, Sq, D)),
            "k_cache": _w((lambda *s: torch.randn(*s, dtype=torch.bfloat16) * 1.6)(B, Hkv, Sk, Dh)),
            "v_cache": _w((lambda *s: torch.randn(*s, dtype=torch.bfloat16) * 0.8)(B, Hkv, Sk, Dh)),
            "wq": _w((lambda *s: torch.randn(*s, dtype=torch.bfloat16) * 1.6)(D, Hq * Dh) * (D ** -0.5)),
            "wo": _w((lambda *s: torch.randn(*s, dtype=torch.bfloat16) * 0.8)(Hq * Dh, D) * ((Hq * Dh) ** -0.5)),
        }
    _A_in = _A_setup()

    def _B_setup():
        B, S, D, F = 1, 1, 640, 1536
        return {
            "x": _w((lambda *s: torch.randn(*s, dtype=torch.bfloat16) * 0.05)(B, S, D)),
            "w1": _w((lambda *s: torch.randn(*s, dtype=torch.bfloat16) * 0.4)(D, F) * (2.0 / D) ** 0.5),
            "w3": _w((lambda *s: torch.randn(*s, dtype=torch.bfloat16) * 0.4)(D, F) * (2.0 / D) ** 0.5),
            "w2": _w((lambda *s: torch.randn(*s, dtype=torch.bfloat16) * 0.4)(F, D) * (2.0 / F) ** 0.5),
        }
    _B_in = _B_setup()

    return {
        "A_x": _A_in["x"],
        "A_k_cache": _A_in["k_cache"],
        "A_v_cache": _A_in["v_cache"],
        "A_wq": _A_in["wq"],
        "A_wo": _A_in["wo"],
        "B_x": _B_in["x"],
        "B_w1": _B_in["w1"],
        "B_w3": _B_in["w3"],
        "B_w2": _B_in["w2"],
    }

def reference(A_x, A_k_cache, A_v_cache, A_wq, A_wo, B_x, B_w1, B_w3, B_w2):
    def _A_ref(x, k_cache, v_cache, wq, wo):
        B, Sq, D = x.shape
        Hq, Dh = 32, 192
        Hkv, Sk = k_cache.shape[1], k_cache.shape[2]
        W = min(4096, max(256, Sk // 4))
        rep = Hq // Hkv
        q = (lambda x, *d: x.permute(*d))((x @ wq).reshape(B, Sq, Hq, Dh), 0, 2, 1, 3)
        k = (lambda x, n, axis: torch.repeat_interleave(x, n, dim=axis))(k_cache[:, :, Sk - W:, :], rep, axis=1)
        v = (lambda x, n, axis: torch.repeat_interleave(x, n, dim=axis))(v_cache[:, :, Sk - W:, :], rep, axis=1)
        scores = torch.einsum("bhqd,bhkd->bhqk", q, k) * (Dh ** -0.5)
        p = _softmax(scores, axis=-1)
        o = torch.einsum("bhqk,bhkd->bhqd", p, v)
        return (lambda x, *d: x.permute(*d))(o, 0, 2, 1, 3).reshape(B, Sq, Hq * Dh) @ wo

    def _B_ref(x, w1, w3, w2):
        gate = torch.einsum("bsd,df->bsf", x, w1)
        up = torch.einsum("bsd,df->bsf", x, w3)
        h = _silu(gate) * up
        out = torch.einsum("bsf,fd->bsd", h, w2)
        return out

    x = A_x
    xn = _layernorm(x, axis=-1)
    return x + _A_ref(xn, A_k_cache, A_v_cache, A_wq, A_wo) + _B_ref(xn, B_w1, B_w3, B_w2)
```

```cpp
#include <hip/hip_runtime.h>
#include <hip/hip_bf16.h>

typedef unsigned v4u __attribute__((ext_vector_type(4)));
typedef float f2 __attribute__((ext_vector_type(2)));
typedef unsigned long long u64;
typedef unsigned short u16;
typedef __attribute__((ext_vector_type(2))) __bf16 bf16x2;
#define GAS __attribute__((address_space(1)))
#define LAS __attribute__((address_space(3)))
typedef GAS u64 gu64;
#define RLX_AGENT __ATOMIC_RELAXED, __HIP_MEMORY_SCOPE_AGENT

#define MIMIC 1

constexpr int DM = 640, NH = 32, SK = 32768, DH = 192, WIN = 4096, FF = 1536;
constexpr int NWG = 256, NTHR = 512;
constexpr int ROWB = DH * 2;
constexpr size_t WS_EPOCH = 0;
constexpr size_t WS_QP = 64;
constexpr size_t WS_OP = WS_QP + (size_t)NWG * 320;
constexpr size_t WS_FP = WS_OP + (size_t)NWG * 256;
constexpr size_t WS_END = WS_FP + (size_t)80 * 2048;
constexpr unsigned SPIN_CAP = 1u << 18;
constexpr float LOG2E = 1.4426950408889634f;

__device__ __forceinline__ float bf_lo(unsigned d) { return __uint_as_float(d << 16); }
__device__ __forceinline__ float bf_hi(unsigned d) { return __uint_as_float(d & 0xffff0000u); }
__device__ __forceinline__ unsigned pk_bf16(float lo, float hi) {
  bf16x2 v = {(__bf16)lo, (__bf16)hi};
  return __builtin_bit_cast(unsigned, v);
}
__device__ __forceinline__ float rbf(float x) { return (float)(__bf16)x; }
__device__ __forceinline__ float dot2(unsigned a, unsigned b, float acc) {
  return __builtin_amdgcn_fdot2_f32_bf16(__builtin_bit_cast(bf16x2, a), __builtin_bit_cast(bf16x2, b), acc, false);
}
template <int CTRL> __device__ __forceinline__ float dpp(float x) {
  return __builtin_bit_cast(float, __builtin_amdgcn_mov_dpp(__builtin_bit_cast(int, x), CTRL, 0xf, 0xf, true));
}
constexpr int XOR1 = 0xB1, XOR2 = 0x4E, XOR7 = 0x141, ROR8 = 0x128, BCAST = 0x150;
__device__ __forceinline__ float swap32_add(float a, float b) {
  auto r = __builtin_amdgcn_permlane32_swap(__float_as_uint(a), __float_as_uint(b), false, false);
  return __uint_as_float(r[0]) + __uint_as_float(r[1]);
}
__device__ __forceinline__ float swap16_add(float a, float b) {
  auto r = __builtin_amdgcn_permlane16_swap(__float_as_uint(a), __float_as_uint(b), false, false);
  return __uint_as_float(r[0]) + __uint_as_float(r[1]);
}
__device__ __forceinline__ float wave_sum(float x) {
  x += dpp<XOR1>(x); x += dpp<XOR2>(x); x += dpp<XOR7>(x); x += dpp<ROR8>(x);
  x = swap16_add(x, x); x = swap32_add(x, x);
  return x;
}
__device__ __forceinline__ float wave_max(float x) {
  x = fmaxf(x, dpp<XOR1>(x)); x = fmaxf(x, dpp<XOR2>(x)); x = fmaxf(x, dpp<XOR7>(x)); x = fmaxf(x, dpp<ROR8>(x));
  auto r = __builtin_amdgcn_permlane16_swap(__float_as_uint(x), __float_as_uint(x), false, false);
  x = fmaxf(__uint_as_float(r[0]), __uint_as_float(r[1]));
  auto t = __builtin_amdgcn_permlane32_swap(__float_as_uint(x), __float_as_uint(x), false, false);
  return fmaxf(__uint_as_float(t[0]), __uint_as_float(t[1]));
}
__device__ __forceinline__ void put_bits(gu64* g, unsigned thi, unsigned tlo, unsigned bits) {
  const unsigned lo = __builtin_amdgcn_alignbit(tlo, bits, 8);
  __hip_atomic_store(g, ((u64)thi << 32) | lo, RLX_AGENT);
}
__device__ __forceinline__ void put_f(gu64* g, unsigned thi, unsigned tlo, float v) { put_bits(g, thi, tlo, __float_as_uint(v)); }
__device__ __forceinline__ u64 get_g(const gu64* g) { return __hip_atomic_load(const_cast<gu64*>(g), RLX_AGENT); }
__device__ __forceinline__ bool g_ok(u64 x, unsigned thi, unsigned tlo) { return ((unsigned)(x >> 32) == thi) & (((unsigned)x >> 24) == tlo); }
__device__ __forceinline__ float g_val(u64 x) { return __uint_as_float((unsigned)x << 8); }
__device__ __forceinline__ v4u ld_nt(__amdgpu_buffer_rsrc_t r, int voff, int soff) { return __builtin_amdgcn_raw_buffer_load_b128(r, voff, soff, 2); }
#define SB() __builtin_amdgcn_sched_barrier(0)
#define PIN(x) asm volatile("" : "+v"(x))

constexpr int VPRE = 4;

__global__ void __launch_bounds__(NTHR, 2)
fused_decode(const u16* __restrict__ xg, const unsigned char* __restrict__ Kc, const unsigned char* __restrict__ Vc,
             const unsigned char* __restrict__ wq, const unsigned char* __restrict__ wo, const unsigned char* __restrict__ w1,
             const unsigned char* __restrict__ w3, const unsigned char* __restrict__ w2, u16* __restrict__ out, u64* __restrict__ wsp) {
  __shared__ __attribute__((aligned(16))) float lds[8 * 192 + 96 + 32 + 16 + 64 + 16];
  float* l_o = lds;
  unsigned* l_q = (unsigned*)(lds + 1536);
  float* l_y = lds + 1536 + 96;
  float* l_ml = lds + 1536 + 128;
  float* l_red = lds + 1536 + 144;
  volatile LAS unsigned* l_flag = (volatile LAS unsigned*)(lds + 1536 + 208);

  const int b = blockIdx.x, tid = threadIdx.x, lane = tid & 63, w = __builtin_amdgcn_readfirstlane(tid >> 6);
  const int h = b >> 3, s = b & 7;
  gu64* ws = (gu64*)wsp;
  __builtin_amdgcn_s_setprio(3);
  int vz = 0; PIN(vz);
  const u64 e0 = get_g(ws + WS_EPOCH + vz);
  if (tid == 0) *l_flag = 0u;

  const size_t kvbase = ((size_t)h * SK + (SK - WIN) + s * 512 + w * 64) * ROWB;
  const __amdgpu_buffer_rsrc_t rk = __builtin_amdgcn_make_buffer_rsrc((void*)(Kc + kvbase), 0, 64 * ROWB, 0x00020000);
  const __amdgpu_buffer_rsrc_t rv = __builtin_amdgcn_make_buffer_rsrc((void*)(Vc + kvbase), 0, 64 * ROWB, 0x00020000);
  const int bit3 = (lane >> 3) & 1;
  const int offA = (lane >> 4) * ROWB + (lane & 15) * 16;
  const int offC = ((lane >> 4) + 4 * bit3) * ROWB + 256 + (lane & 7) * 16;
  const float xres = __uint_as_float((unsigned)xg[(b < 80 ? b : 0) * 8 + (lane & 7)] << 16);

  unsigned thi, tlo;
  u64 e1;
  v4u kr[24];
  if (w < 5) {
    const int rg = lane >> 3, c = lane & 7;
    unsigned xa[5], xr[5];
    const unsigned* x32 = (const unsigned*)xg;
#pragma unroll
    for (int i = 0; i < 5; ++i) xa[i] = x32[lane + 64 * i];
#pragma unroll
    for (int i = 0; i < 5; ++i) xr[i] = x32[40 * s + 5 * rg + i];
    const unsigned char* wb = w < 3 ? wq + (size_t)h * 384 + w * 128 : (w == 3 ? w1 : w3) + (size_t)h * 96;
    const int stride = w < 3 ? 12288 : 3072;
    const int seg = w < 3 ? 128 : 96;
    const __amdgpu_buffer_rsrc_t rw = __builtin_amdgcn_make_buffer_rsrc((void*)(wb + (size_t)(80 * s) * stride), 0, 79 * stride + seg, 0x00020000);
    const int woff = (c * 16 < seg) ? rg * 10 * stride + c * 16 : 0x7fffff00;
    v4u wr[10];
#pragma unroll
    for (int i = 0; i < 10; ++i) wr[i] = ld_nt(rw, woff, i * stride);
    SB();
    asm volatile("s_waitcnt lgkmcnt(0)\n\ts_barrier" ::: "memory");
    SB();
    float sm = 0.f;
#pragma unroll
    for (int i = 0; i < 5; ++i) sm += bf_lo(xa[i]) + bf_hi(xa[i]);
    const float mean = wave_sum(sm) * (1.0f / DM);
    float sq = 0.f;
#pragma unroll
    for (int i = 0; i < 5; ++i) { const float a = bf_lo(xa[i]) - mean, c2 = bf_hi(xa[i]) - mean; sq += a * a + c2 * c2; }
    const float var = wave_sum(sq) * (1.0f / DM);
    float xn[10];
    const float mb = rbf(mean), den = rbf(sqrtf(rbf(rbf(var) + 1e-5f)));
#pragma unroll
    for (int i = 0; i < 5; ++i) { xn[2 * i] = rbf(rbf(bf_lo(xr[i]) - mb) / den); xn[2 * i + 1] = rbf(rbf(bf_hi(xr[i]) - mb) / den); }
    e1 = e0 * 6364136223846793005ull + 1442695040888963407ull;
    thi = (unsigned)(e1 >> 8); tlo = (unsigned)e1 & 0xffu;
    float acc[8];
#pragma unroll
    for (int j = 0; j < 8; ++j) acc[j] = 0.f;
#pragma unroll
    for (int i = 0; i < 10; ++i)
#pragma unroll
      for (int k = 0; k < 4; ++k) { acc[2 * k] = fmaf(xn[i], bf_lo(wr[i][k]), acc[2 * k]); acc[2 * k + 1] = fmaf(xn[i], bf_hi(wr[i][k]), acc[2 * k + 1]); }
    float a4[4], a2[2];
#pragma unroll
    for (int j = 0; j < 4; ++j) a4[j] = swap32_add(acc[j], acc[j + 4]);
#pragma unroll
    for (int j = 0; j < 2; ++j) a2[j] = swap16_add(a4[j], a4[j + 2]);
    const float t0 = a2[0] + dpp<ROR8>(a2[0]), t1 = a2[1] + dpp<ROR8>(a2[1]);
    const float tot = bit3 ? t1 : t0;
    put_f(ws + WS_QP + (size_t)b * 320 + w * 64 + 8 * c + rg, thi, tlo, tot);
    SB();
    if (w == 0) {
      const gu64* qp = ws + WS_QP + (size_t)h * 8 * 320;
      u64 g[24], gg, gu;
      const int fo = (lane & 7) * 320 + 192 + 6 * s + min(lane >> 3, 5);
      unsigned done = 0;
      for (unsigned spin = 0; spin < SPIN_CAP; ++spin) {
#pragma unroll
        for (int p = 0; p < 8; ++p)
          if (!((done >> p) & 1)) {
#pragma unroll
            for (int j = 0; j < 3; ++j) g[p * 3 + j] = get_g(qp + p * 320 + j * 64 + lane);
          }
        if (!((done >> 8) & 1)) { gg = get_g(qp + fo); gu = get_g(qp + fo + 64); }
#pragma unroll
        for (int p = 0; p < 8; ++p)
          if (!((done >> p) & 1)) {
            const bool ok = g_ok(g[p * 3], thi, tlo) & g_ok(g[p * 3 + 1], thi, tlo) & g_ok(g[p * 3 + 2], thi, tlo);
            if (__all(ok)) done |= 1u << p;
          }
        if (!((done >> 8) & 1)) { if (__all(g_ok(gg, thi, tlo) & g_ok(gu, thi, tlo))) done |= 256u; }
        if (done == 511u) break;
        __builtin_amdgcn_s_sleep(4);
      }
      float qv[3];
#pragma unroll
      for (int j = 0; j < 3; ++j) {
        float t = 0.f;
#pragma unroll
        for (int p = 0; p < 8; ++p) t += g_val(g[p * 3 + j]);
        qv[j] = t;
      }
#pragma unroll
      for (int j = 0; j < 3; ++j) ((LAS __bf16*)l_q)[j * 64 + lane] = (__bf16)qv[j];
      float gt = g_val(gg), up = g_val(gu);
      gt += dpp<XOR1>(gt); gt += dpp<XOR2>(gt); gt += dpp<XOR7>(gt);
      up += dpp<XOR1>(up); up += dpp<XOR2>(up); up += dpp<XOR7>(up);
      gt = rbf(gt); up = rbf(up);
      const float hv = rbf(rbf(gt / (1.0f + __expf(-gt))) * up);
      if ((lane & 7) == 0 && lane < 48) l_y[24 + (lane >> 3)] = hv;
      if (lane >= 62) l_y[30 + (lane & 1)] = 0.f;
      asm volatile("s_waitcnt lgkmcnt(0)" ::: "memory");
      *l_flag = 1u;
    }
  } else {
    SB();
  }
#pragma unroll
  for (int g = 0; g < 8; ++g) {
    kr[3 * g] = ld_nt(rk, offA, g * 3072);
    kr[3 * g + 1] = ld_nt(rk, offA, g * 3072 + 1536);
    kr[3 * g + 2] = ld_nt(rk, offC, g * 3072);
    SB();
    if (g == 0 && w >= 5) { asm volatile("s_barrier" ::: "memory"); SB(); }
  }
  SB();
  __builtin_amdgcn_s_setprio(1);
  if (w >= 5) {
    e1 = e0 * 6364136223846793005ull + 1442695040888963407ull;
    thi = (unsigned)(e1 >> 8); tlo = (unsigned)e1 & 0xffu;
  }
  v4u vr[24];
#pragma unroll
  for (int g = 0; g < VPRE; ++g) {
    vr[3 * g] = ld_nt(rv, offA, g * 3072);
    vr[3 * g + 1] = ld_nt(rv, offA, g * 3072 + 1536);
    vr[3 * g + 2] = ld_nt(rv, offC, g * 3072);
    SB();
  }
  SB();
  while (*l_flag == 0u) __builtin_amdgcn_s_sleep(2);
  SB();
  const v4u qa = *(const LAS v4u*)(l_q + (lane & 15) * 4);
  const v4u qc = *(const LAS v4u*)(l_q + 64 + (lane & 7) * 4);
  float xs[8];
#pragma unroll
  for (int g = 0; g < 8; ++g) {
    float sa = 0.f, sb = 0.f, sc = 0.f;
#pragma unroll
    for (int k = 0; k < 4; ++k) { sa = dot2(kr[3 * g][k], qa[k], sa); sb = dot2(kr[3 * g + 1][k], qa[k], sb); sc = dot2(kr[3 * g + 2][k], qc[k], sc); }
    const float keep = bit3 ? sb : sa, send = bit3 ? sa : sb;
    xs[g] = keep + sc + dpp<ROR8>(send);
  }
  float x4[4], x2[2];
#pragma unroll
  for (int j = 0; j < 4; ++j) { const float keep = (lane & 4) ? xs[j + 4] : xs[j], send = (lane & 4) ? xs[j] : xs[j + 4]; x4[j] = keep + dpp<XOR7>(send); }
#pragma unroll
  for (int j = 0; j < 2; ++j) { const float keep = (lane & 2) ? x4[j + 2] : x4[j], send = (lane & 2) ? x4[j] : x4[j + 2]; x2[j] = keep + dpp<XOR2>(send); }
  float sco = ((lane & 1) ? x2[1] : x2[0]) + dpp<XOR1>((lane & 1) ? x2[0] : x2[1]);
  SB();
  __builtin_amdgcn_s_setprio(0);
#pragma unroll
  for (int g = VPRE; g < 8; ++g) {
    vr[3 * g] = ld_nt(rv, offA, g * 3072);
    vr[3 * g + 1] = ld_nt(rv, offA, g * 3072 + 1536);
    vr[3 * g + 2] = ld_nt(rv, offC, g * 3072);
    SB();
  }
  unsigned wd[30];
  {
    const __amdgpu_buffer_rsrc_t ro = __builtin_amdgcn_make_buffer_rsrc((void*)(wo + (size_t)(192 * h + 24 * s) * 1280), 0, 24 * 1280, 0x00020000);
    const __amdgpu_buffer_rsrc_t r2 = __builtin_amdgcn_make_buffer_rsrc((void*)(w2 + (size_t)(48 * h + 6 * s) * 1280), 0, 6 * 1280, 0x00020000);
    const int po = tid < 320 ? tid * 4 : 0x7fffff00;
#pragma unroll
    for (int i = 0; i < 24; ++i) wd[i] = __builtin_amdgcn_raw_buffer_load_b32(ro, po, i * 1280, 2);
#pragma unroll
    for (int i = 0; i < 6; ++i) wd[24 + i] = __builtin_amdgcn_raw_buffer_load_b32(r2, po, i * 1280, 2);
  }
  SB();
  sco = rbf(rbf(sco) * 0.07216878364870322f) * LOG2E;
  const float mw = wave_max(sco);
  const float p = __builtin_amdgcn_exp2f(sco - mw);
  const float lw = wave_sum(p);
  if (lane == 0) { l_ml[2 * w] = mw; l_ml[2 * w + 1] = lw; }
  float oa[8], oc[8];
#pragma unroll
  for (int j = 0; j < 8; ++j) { oa[j] = 0.f; oc[j] = 0.f; }
#define PV_G(G) { \
    const float pa = dpp<BCAST + (G)>(p), pb = dpp<BCAST + 8 + (G)>(p); const float pc = bit3 ? pb : pa; \
    _Pragma("unroll") for (int k = 0; k < 4; ++k) { \
      oa[2 * k] = fmaf(pa, bf_lo(vr[3 * (G)][k]), oa[2 * k]); oa[2 * k + 1] = fmaf(pa, bf_hi(vr[3 * (G)][k]), oa[2 * k + 1]); \
      oa[2 * k] = fmaf(pb, bf_lo(vr[3 * (G) + 1][k]), oa[2 * k]); oa[2 * k + 1] = fmaf(pb, bf_hi(vr[3 * (G) + 1][k]), oa[2 * k + 1]); \
      oc[2 * k] = fmaf(pc, bf_lo(vr[3 * (G) + 2][k]), oc[2 * k]); oc[2 * k + 1] = fmaf(pc, bf_hi(vr[3 * (G) + 2][k]), oc[2 * k + 1]); } \
    SB(); }
  PV_G(0) PV_G(1) PV_G(2) PV_G(3) PV_G(4) PV_G(5) PV_G(6) PV_G(7)
#undef PV_G
  {
    float a4[4], c4[4], a2[2], c2[2];
#pragma unroll
    for (int j = 0; j < 4; ++j) { a4[j] = swap32_add(oa[j], oa[j + 4]); c4[j] = swap32_add(oc[j], oc[j + 4]); }
#pragma unroll
    for (int j = 0; j < 2; ++j) { a2[j] = swap16_add(a4[j], a4[j + 2]); c2[j] = swap16_add(c4[j], c4[j + 2]); }
    const float cc = (bit3 ? c2[1] : c2[0]) + dpp<ROR8>(bit3 ? c2[0] : c2[1]);
    float* lo = l_o + w * 192;
    *(f2*)(lo + 8 * (lane & 15) + 2 * (lane >> 4)) = f2{a2[0], a2[1]};
    lo[128 + 8 * (lane & 7) + (lane >> 3)] = cc;
  }
  __syncthreads();
  if (tid < 256) {
    const int cs = tid >> 5, j = tid & 31;
    float M = l_ml[0];
#pragma unroll
    for (int i = 1; i < 8; ++i) M = fmaxf(M, l_ml[2 * i]);
    float L = 0.f, o = 0.f;
    const int d = 24 * cs + min(j, 23);
#pragma unroll
    for (int i = 0; i < 8; ++i) { const float sc = __builtin_amdgcn_exp2f(l_ml[2 * i] - M); L = fmaf(sc, l_ml[2 * i + 1], L); o = fmaf(sc, l_o[i * 192 + d], o); }
    const unsigned bits = j < 24 ? __float_as_uint(o) : j == 24 ? __float_as_uint(M) : j == 25 ? (__float_as_uint(M) << 24) : __float_as_uint(L);
    if (j < 27) put_bits(ws + WS_OP + ((size_t)(h * 8 + cs) * 8 + s) * 32 + j, thi, tlo, bits);
  }
  float wl[30], wh[30];
#pragma unroll
  for (int i = 0; i < 30; ++i) { wl[i] = bf_lo(wd[i]); wh[i] = bf_hi(wd[i]); PIN(wl[i]); PIN(wh[i]); }
  if (w == 7) {
    const gu64* op = ws + WS_OP + (size_t)b * 256;
    const int j = lane & 31;
    u64 g[4];
    for (unsigned spin = 0; spin < SPIN_CAP; ++spin) {
      bool ok = true;
#pragma unroll
      for (int k = 0; k < 4; ++k) g[k] = get_g(op + k * 64 + (lane & 32) + min(j, 26));
#pragma unroll
      for (int k = 0; k < 4; ++k) ok = ok & g_ok(g[k], thi, tlo);
      if (__all(ok)) break;
      __builtin_amdgcn_s_sleep(1);
    }
    float Mp[8], Lp[8];
#pragma unroll
    for (int k = 0; k < 4; ++k)
#pragma unroll
      for (int hf = 0; hf < 2; ++hf) {
        const unsigned lo = (unsigned)g[k];
        const unsigned mh = __builtin_amdgcn_readlane(lo, 32 * hf + 24), ml = __builtin_amdgcn_readlane(lo, 32 * hf + 25), ll = __builtin_amdgcn_readlane(lo, 32 * hf + 26);
        Mp[2 * k + hf] = __uint_as_float((mh << 8) | ((ml >> 16) & 0xffu));
        Lp[2 * k + hf] = __uint_as_float(ll << 8);
      }
    float M = Mp[0];
#pragma unroll
    for (int i = 1; i < 8; ++i) M = fmaxf(M, Mp[i]);
    float L = 0.f, cf[8];
#pragma unroll
    for (int i = 0; i < 8; ++i) { cf[i] = __builtin_amdgcn_exp2f(Mp[i] - M); L = fmaf(cf[i], Lp[i], L); }
    float o = 0.f;
#pragma unroll
    for (int k = 0; k < 4; ++k) { float c0 = cf[2 * k], c1 = cf[2 * k + 1]; PIN(c0); PIN(c1); o = fmaf(lane < 32 ? c0 : c1, g_val(g[k]), o); }
    o = swap32_add(o, o) / L;
    o = rbf(o);
    if (lane < 24) l_y[lane] = o;
  }
  __syncthreads();
  if (w < 5) {
    float a0 = 0.f, a1 = 0.f;
#pragma unroll
    for (int i = 0; i < 30; ++i) { const float y = l_y[i]; a0 = fmaf(y, wl[i], a0); a1 = fmaf(y, wh[i], a1); }
    gu64* fp = ws + WS_FP + ((size_t)(tid >> 2) * 256 + b) * 8 + (tid & 3) * 2;
    put_f(fp, thi, tlo, a0);
    put_f(fp + 1, thi, tlo, a1);
  }
  if (b >= 80) return;
  {
    const gu64* fp = ws + WS_FP + (size_t)b * 2048 + tid;
    u64 g[4];
    for (unsigned spin = 0; spin < SPIN_CAP; ++spin) {
      bool ok = true;
#pragma unroll
      for (int k = 0; k < 4; ++k) g[k] = get_g(fp + k * 512);
#pragma unroll
      for (int k = 0; k < 4; ++k) ok = ok & g_ok(g[k], thi, tlo);
      if (__all(ok)) break;
      __builtin_amdgcn_s_sleep(1);
    }
    float t = (g_val(g[0]) + g_val(g[1])) + (g_val(g[2]) + g_val(g[3]));
    t += dpp<ROR8>(t);
    t = swap16_add(t, t);
    t = swap32_add(t, t);
    if (lane < 8) l_red[w * 8 + lane] = t;
    __syncthreads();
    if (tid < 8) {
      float r = 0.f;
#pragma unroll
      for (int i = 0; i < 8; ++i) r += l_red[i * 8 + tid];
      r += xres;
      out[b * 8 + tid] = __builtin_bit_cast(u16, (__bf16)r);
      if (b == 0 && tid == 0) __hip_atomic_store(ws + WS_EPOCH, e1, RLX_AGENT);
    }
  }
}

extern "C" void kernel_launch(void* const* d_in, const int* in_sizes, int n_in, void* d_out, int out_size, void* d_ws, size_t ws_size,
                              hipStream_t stream) {
  (void)in_sizes; (void)n_in; (void)out_size; (void)ws_size;
  fused_decode<<<dim3(NWG), dim3(NTHR), 0, stream>>>((const u16*)d_in[0], (const unsigned char*)d_in[1], (const unsigned char*)d_in[2],
                                                      (const unsigned char*)d_in[3], (const unsigned char*)d_in[4], (const unsigned char*)d_in[6],
                                                      (const unsigned char*)d_in[7], (const unsigned char*)d_in[8], (u16*)d_out, (u64*)d_ws);
}
```

```cpp
#include <hip/hip_runtime.h>
#include <hip/hip_bf16.h>

typedef unsigned v4u __attribute__((ext_vector_type(4)));
typedef float f2 __attribute__((ext_vector_type(2)));
typedef unsigned long long u64;
typedef unsigned short u16;
typedef __attribute__((ext_vector_type(2))) __bf16 bf16x2;
#define GAS __attribute__((address_space(1)))
#define LAS __attribute__((address_space(3)))
typedef GAS u64 gu64;
#define RLX_AGENT __ATOMIC_RELAXED, __HIP_MEMORY_SCOPE_AGENT

#define MIMIC 1
#define WO_LATE 0
#define SLEEP_LONG 4
#define SLEEP_SHORT 0
#define ABL 0
#define NFIX 8
#define QDELAY 8
#define FP_PAD 16

constexpr int DM = 640, SK = 32768, WIN = 4096;
constexpr int NWG = 256, NTHR = 1024;
constexpr int ROWB = 384;
constexpr size_t WS_EPOCH = 0;
constexpr size_t WS_QP = 64;
constexpr size_t WS_GP = WS_QP + (size_t)NWG * 192;
constexpr size_t WS_OP = WS_GP + (size_t)240 * 128;
constexpr size_t WS_ST = WS_OP + (size_t)NWG * 192;
constexpr size_t WS_FP = WS_ST + (size_t)NWG * 4;
constexpr size_t WS_CN = WS_FP + (size_t)80 * 24 * FP_PAD;
constexpr size_t WS_END = WS_CN + 32 * 32;
constexpr unsigned SPIN_CAP = 1u << 18;
constexpr float LOG2E = 1.4426950408889634f;
constexpr int OOB = 0x7fffff00;

__device__ __forceinline__ float bf_lo(unsigned d) { return __uint_as_float(d << 16); }
__device__ __forceinline__ float bf_hi(unsigned d) { return __uint_as_float(d & 0xffff0000u); }
__device__ __forceinline__ float rbf(float x) { return (float)(__bf16)x; }
__device__ __forceinline__ float dot2(unsigned a, unsigned b, float acc) {
  return __builtin_amdgcn_fdot2_f32_bf16(__builtin_bit_cast(bf16x2, a), __builtin_bit_cast(bf16x2, b), acc, false);
}
template <int CTRL> __device__ __forceinline__ float dpp(float x) {
  return __builtin_bit_cast(float, __builtin_amdgcn_mov_dpp(__builtin_bit_cast(int, x), CTRL, 0xf, 0xf, true));
}
constexpr int XOR1 = 0xB1, XOR2 = 0x4E, XOR7 = 0x141, ROR8 = 0x128, BCAST = 0x150;
__device__ __forceinline__ float swap32_add(float a, float b) {
  auto r = __builtin_amdgcn_permlane32_swap(__float_as_uint(a), __float_as_uint(b), false, false);
  return __uint_as_float(r[0]) + __uint_as_float(r[1]);
}
__device__ __forceinline__ float swap16_add(float a, float b) {
  auto r = __builtin_amdgcn_permlane16_swap(__float_as_uint(a), __float_as_uint(b), false, false);
  return __uint_as_float(r[0]) + __uint_as_float(r[1]);
}
__device__ __forceinline__ float wave_sum(float x) {
  x += dpp<XOR1>(x); x += dpp<XOR2>(x); x += dpp<XOR7>(x); x += dpp<ROR8>(x);
  x = swap16_add(x, x); x = swap32_add(x, x);
  return x;
}
__device__ __forceinline__ float wave_max(float x) {
  x = fmaxf(x, dpp<XOR1>(x)); x = fmaxf(x, dpp<XOR2>(x)); x = fmaxf(x, dpp<XOR7>(x)); x = fmaxf(x, dpp<ROR8>(x));
  auto r = __builtin_amdgcn_permlane16_swap(__float_as_uint(x), __float_as_uint(x), false, false);
  x = fmaxf(__uint_as_float(r[0]), __uint_as_float(r[1]));
  auto t = __builtin_amdgcn_permlane32_swap(__float_as_uint(x), __float_as_uint(x), false, false);
  return fmaxf(__uint_as_float(t[0]), __uint_as_float(t[1]));
}
__device__ __forceinline__ float treduce8(const float (&acc)[8], int bit3) {
  float a4[4], a2[2];
#pragma unroll
  for (int j = 0; j < 4; ++j) a4[j] = swap32_add(acc[j], acc[j + 4]);
#pragma unroll
  for (int j = 0; j < 2; ++j) a2[j] = swap16_add(a4[j], a4[j + 2]);
  return (bit3 ? a2[1] : a2[0]) + dpp<ROR8>(bit3 ? a2[0] : a2[1]);
}
__device__ __forceinline__ void put_bits(gu64* g, unsigned thi, unsigned tlo, unsigned bits) {
  const unsigned lo = __builtin_amdgcn_alignbit(tlo, bits, 8);
  __hip_atomic_store(g, ((u64)thi << 32) | lo, RLX_AGENT);
}
__device__ __forceinline__ void put_f(gu64* g, unsigned thi, unsigned tlo, float v) { put_bits(g, thi, tlo, __float_as_uint(v)); }
__device__ __forceinline__ u64 get_g(const gu64* g) { return __hip_atomic_load(const_cast<gu64*>(g), RLX_AGENT); }
__device__ __forceinline__ u64 get_rmw(gu64* g) { u64 z = 0; asm volatile("" : "+v"(z)); return __hip_atomic_fetch_or(g, z, RLX_AGENT); }
__device__ __forceinline__ bool g_ok(u64 x, unsigned thi, unsigned tlo) { return ((unsigned)(x >> 32) == thi) & (((unsigned)x >> 24) == tlo); }
__device__ __forceinline__ float g_val(u64 x) { return __uint_as_float((unsigned)x << 8); }
__device__ __forceinline__ u64 next_epoch(u64 e0) {
  const u64 e = ((u64)(unsigned)__builtin_amdgcn_readfirstlane((unsigned)(e0 >> 32)) << 32) | (unsigned)__builtin_amdgcn_readfirstlane((unsigned)e0);
  return e * 6364136223846793005ull + 1442695040888963407ull;
}
__device__ __forceinline__ v4u ld_nt(__amdgpu_buffer_rsrc_t r, int voff, int soff) { return __builtin_amdgcn_raw_buffer_load_b128(r, voff, soff, 2); }
#define SB() __builtin_amdgcn_sched_barrier(0)
#define PIN(x) asm volatile("" : "+v"(x))
#define LDS_FENCE() asm volatile("s_waitcnt lgkmcnt(0)" ::: "memory")

#define STAMP(i) do { } while (0)
#define STAMP_FLUSH() do { } while (0)

constexpr int F_DONE = 0, F_Q = 32, F_PUB = 36, F_X = 40, F_CNT = 41, F_EMPTY = 42;

__device__ __forceinline__ void wait_flags(volatile LAS unsigned* f, int n, int lane, int sleep) {
  const int i = lane < n ? lane : 0;
  while (!__all(f[i] != 0u)) { if (sleep) __builtin_amdgcn_s_sleep(SLEEP_LONG); else __builtin_amdgcn_s_sleep(SLEEP_SHORT); }
  asm volatile("" ::: "memory");
}

__global__ void __launch_bounds__(NTHR, 4)
fused_decode(const u16* __restrict__ xg, const unsigned char* __restrict__ Kc, const unsigned char* __restrict__ Vc,
             const unsigned char* __restrict__ wq, const unsigned char* __restrict__ wo, const unsigned char* __restrict__ w1,
             const unsigned char* __restrict__ w3, const unsigned char* __restrict__ w2, u16* __restrict__ out, u64* __restrict__ wsp) {
  __shared__ __attribute__((aligned(16))) float lds[16 * 192 + 96 + 32 + 320 + 648 + 64 + 4];
  float* l_o = lds;
  unsigned* l_q = (unsigned*)(lds + 3072);
  float* l_ml = lds + 3072 + 96;
  float* l_gv = lds + 3072 + 128;
  float* l_xn = lds + 3072 + 448;
  volatile LAS unsigned* l_f = (volatile LAS unsigned*)(lds + 3072 + 1096);
  volatile LAS unsigned* l_tag = (volatile LAS unsigned*)(lds + 3072 + 1160);

  const int b = blockIdx.x, tid = threadIdx.x, lane = tid & 63, w = __builtin_amdgcn_readfirstlane(tid >> 6);
  const int h = b >> 3, s = b & 7;
  const int rgp = b / 10, cc = b - rgp * 10;
  gu64* ws = (gu64*)wsp;
  STAMP(0);
  const size_t kvbase = ((size_t)h * SK + (SK - WIN)) * ROWB;
  const __amdgpu_buffer_rsrc_t rk = __builtin_amdgcn_make_buffer_rsrc((void*)(Kc + kvbase), 0, WIN * ROWB, 0x00020000);
  const __amdgpu_buffer_rsrc_t rv = __builtin_amdgcn_make_buffer_rsrc((void*)(Vc + kvbase), 0, WIN * ROWB, 0x00020000);
  const int bit3 = (lane >> 3) & 1, rg = lane >> 3, c8 = lane & 7;
  const int offA = (lane >> 4) * ROWB + (lane & 15) * 16;
  const int offC = ((lane >> 4) + 4 * bit3) * ROWB + 256 + c8 * 16;

  __builtin_amdgcn_s_setprio(1);
  if (w < 6) {
    if (w == 0) {
      const v4u xa = *(const v4u*)(xg + lane * 8);
      const unsigned xb = *(const unsigned*)(xg + 512 + lane * 2);
      int vz = 0; PIN(vz);
      const u64 e0 = get_g(ws + WS_EPOCH + vz);
      l_f[lane] = 0u;
      SB();
      asm volatile("s_waitcnt lgkmcnt(0)\n\ts_barrier" ::: "memory");
      SB();
      float xv[10];
#pragma unroll
      for (int i = 0; i < 4; ++i) { xv[2 * i] = bf_lo(xa[i]); xv[2 * i + 1] = bf_hi(xa[i]); }
      xv[8] = bf_lo(xb); xv[9] = bf_hi(xb);
      float sm = 0.f;
#pragma unroll
      for (int i = 0; i < 10; ++i) sm += xv[i];
      const float mean = wave_sum(sm) * (1.0f / DM);
      float sq = 0.f;
#pragma unroll
      for (int i = 0; i < 10; ++i) { const float a = xv[i] - mean; sq = fmaf(a, a, sq); }
      const float var = wave_sum(sq) * (1.0f / DM);
      const float mb = rbf(mean), rs = 1.0f / rbf(sqrtf(rbf(rbf(var) + 1e-5f)));
#pragma unroll
      for (int i = 0; i < 10; ++i) xv[i] = rbf(rbf(xv[i] - mb) * rs);
#pragma unroll
      for (int i = 0; i < 4; ++i) *(LAS f2*)(l_xn + lane * 8 + 2 * i) = f2{xv[2 * i], xv[2 * i + 1]};
      *(LAS f2*)(l_xn + 512 + lane * 2) = f2{xv[8], xv[9]};
      const u64 e1 = next_epoch(e0);
      if (lane == 0) { l_tag[0] = (unsigned)e1; l_tag[1] = (unsigned)(e1 >> 32); }
      LDS_FENCE();
      l_f[F_X] = 1u;
      SB();
      if (s == 0 && lane == 0) put_bits(ws + WS_CN + h * 32, (unsigned)(e1 >> 8), (unsigned)e1 & 0xffu, 0u);
    } else {
      const bool isq = w < 4;
      if (isq || b < 240) {
        const int nr = isq ? 10 : 8, row0 = isq ? 80 * s : 64 * cc, stride = isq ? 12288 : 3072;
        const unsigned char* wb = isq ? wq + h * 384 + (w - 1) * 128 : (w == 4 ? w1 : w3) + rgp * 128;
        const __amdgpu_buffer_rsrc_t rw = __builtin_amdgcn_make_buffer_rsrc((void*)(wb + (size_t)row0 * stride), 0, (8 * nr - 1) * stride + 128, 0x00020000);
        const int woff = rg * nr * stride + c8 * 16, woff2 = isq ? woff : OOB;
        v4u wr[10];
#pragma unroll
        for (int i = 0; i < 10; ++i) { wr[i] = ld_nt(rw, i < 8 ? woff : woff2, i * stride); if (i == 1) { SB(); asm volatile("s_barrier" ::: "memory"); SB(); } }
        SB();
        wait_flags(l_f + F_X, 1, lane, 0);
        f2 xn[5];
#pragma unroll
        for (int i = 0; i < 5; ++i) xn[i] = *(const LAS f2*)(l_xn + row0 + nr * rg + 2 * i);
        if (!isq) xn[4] = f2{0.f, 0.f};
        const unsigned t0 = __builtin_amdgcn_readfirstlane(l_tag[0]), t1 = __builtin_amdgcn_readfirstlane(l_tag[1]);
        const unsigned thi = (t0 >> 8) | (t1 << 24), tlo = t0 & 0xffu;
        float acc[8];
#pragma unroll
        for (int j = 0; j < 8; ++j) acc[j] = 0.f;
#pragma unroll
        for (int i = 0; i < 10; ++i)
#pragma unroll
          for (int k = 0; k < 4; ++k) { acc[2 * k] = fmaf(xn[i >> 1][i & 1], bf_lo(wr[i][k]), acc[2 * k]); acc[2 * k + 1] = fmaf(xn[i >> 1][i & 1], bf_hi(wr[i][k]), acc[2 * k + 1]); }
        const float tot = treduce8(acc, bit3);
        gu64* dst = ws + (isq ? WS_QP + (size_t)((h * 3 + w - 1) * 8 + s) * 64 : WS_GP + (size_t)b * 128 + (w - 4) * 64);
        put_f(dst + 8 * c8 + rg, thi, tlo, tot);
        SB();
        STAMP(9);
        if (isq) {
          const gu64* qp = ws + WS_QP + (size_t)(h * 3 + w - 1) * 512 + lane;
          const gu64* cp = w == 1 ? ws + WS_CN + h * 32 : qp;
          u64 g[8], gc;
          unsigned done = 0;
          __builtin_amdgcn_s_sleep(QDELAY);
          for (unsigned spin = 0; spin < SPIN_CAP; ++spin) {
#pragma unroll
            for (int p = 0; p < 8; ++p) if (!((done >> p) & 1)) g[p] = get_g(qp + p * 64);
            if (!((done >> 8) & 1)) gc = get_g(cp);
#pragma unroll
            for (int p = 0; p < 8; ++p) if (!((done >> p) & 1)) { if (__all(g_ok(g[p], thi, tlo))) done |= 1u << p; }
            if (!((done >> 8) & 1)) { if (__all(g_ok(gc, thi, tlo))) done |= 256u; }
            if (done == 511u) break;
          }
          float t = 0.f;
#pragma unroll
          for (int p = 0; p < 8; ++p) t += g_val(g[p]);
          ((LAS __bf16*)l_q)[(w - 1) * 64 + lane] = (__bf16)t;
          LDS_FENCE();
          l_f[F_Q + w - 1] = 1u;
        }
      } else {
        asm volatile("s_barrier" ::: "memory");
      }
    }
    STAMP(1);
  } else {
    asm volatile("s_barrier" ::: "memory");
  }
  unsigned e1lo = 0, e1hi = 0, thi = 0, tlo = 0;
  {
    int task = s * 16 + w;
    float mrun = -INFINITY, lrun = 0.f, oa[8], oc[8];
#pragma unroll
    for (int j = 0; j < 8; ++j) { oa[j] = 0.f; oc[j] = 0.f; }
    v4u qa = {0u, 0u, 0u, 0u}, qc = {0u, 0u, 0u, 0u};
    const __amdgpu_buffer_rsrc_t rc = __builtin_amdgcn_make_buffer_rsrc((void*)(wsp + WS_CN + h * 32), 0, 8, 0x00020000);
    bool first = true;
#pragma nounroll
    for (;;) {
      v4u kr[6], vr[6];
      const int so = task * (16 * ROWB);
#pragma unroll
      for (int g = 0; g < 2; ++g) {
        kr[3 * g] = ld_nt(rk, offA, so + g * 3072);
        kr[3 * g + 1] = ld_nt(rk, offA, so + g * 3072 + 1536);
        kr[3 * g + 2] = ld_nt(rk, offC, so + g * 3072);
        SB();
      }
#pragma unroll
      for (int g = 0; g < 2; ++g) {
        vr[3 * g] = ld_nt(rv, offA, so + g * 3072);
        vr[3 * g + 1] = ld_nt(rv, offA, so + g * 3072 + 1536);
        vr[3 * g + 2] = ld_nt(rv, offC, so + g * 3072);
        SB();
      }
      if (first) {
        first = false;
        STAMP(2);
        wait_flags(l_f + F_Q, 3, lane, 1);
        e1lo = __builtin_amdgcn_readfirstlane(l_tag[0]); e1hi = __builtin_amdgcn_readfirstlane(l_tag[1]);
        thi = (e1lo >> 8) | (e1hi << 24); tlo = e1lo & 0xffu;
        qa = *(const LAS v4u*)(l_q + (lane & 15) * 4);
        qc = *(const LAS v4u*)(l_q + 64 + c8 * 4);
        STAMP(3);
      }
      float xs[2];
#pragma unroll
      for (int g = 0; g < 2; ++g) {
        float sa = 0.f, sb = 0.f, sc = 0.f;
#pragma unroll
        for (int k = 0; k < 4; ++k) { sa = dot2(kr[3 * g][k], qa[k], sa); sb = dot2(kr[3 * g + 1][k], qa[k], sb); sc = dot2(kr[3 * g + 2][k], qc[k], sc); }
        const float keep = bit3 ? sb : sa, send = bit3 ? sa : sb;
        xs[g] = keep + sc + dpp<ROR8>(send);
      }
      SB();
      const bool more = w >= NFIX && __builtin_amdgcn_readfirstlane(l_f[F_EMPTY]) == 0u;
      const int claim = __builtin_amdgcn_raw_ptr_buffer_atomic_add_i32(1, rc, (more && lane == 0) ? 0 : OOB, 0, 0);
      SB();
      float sco = ((lane & 4) ? xs[1] : xs[0]) + dpp<XOR7>((lane & 4) ? xs[0] : xs[1]);
      sco += dpp<XOR2>(sco);
      sco += dpp<XOR1>(sco);
      sco = rbf(rbf(sco) * 0.07216878364870322f) * LOG2E;
      float mw = fmaxf(sco, dpp<XOR7>(sco));
      mw = fmaxf(mw, dpp<ROR8>(mw));
      {
        auto r = __builtin_amdgcn_permlane16_swap(__float_as_uint(mw), __float_as_uint(mw), false, false);
        mw = fmaxf(__uint_as_float(r[0]), __uint_as_float(r[1]));
        auto t = __builtin_amdgcn_permlane32_swap(__float_as_uint(mw), __float_as_uint(mw), false, false);
        mw = fmaxf(fmaxf(__uint_as_float(t[0]), __uint_as_float(t[1])), mrun);
      }
      const float alpha = __builtin_amdgcn_exp2f(mrun - mw);
      const float p = __builtin_amdgcn_exp2f(sco - mw);
      mrun = mw;
      lrun = fmaf(lrun, alpha, p);
#pragma unroll
      for (int j = 0; j < 8; ++j) { oa[j] *= alpha; oc[j] *= alpha; }
      SB();
#define PV_G(G) { \
      const float pa = dpp<BCAST + 4 * (G)>(p), pb = dpp<BCAST + 8 + 4 * (G)>(p); const float pc = bit3 ? pb : pa; \
      _Pragma("unroll") for (int k = 0; k < 4; ++k) { \
        oa[2 * k] = fmaf(pa, bf_lo(vr[3 * (G)][k]), oa[2 * k]); oa[2 * k + 1] = fmaf(pa, bf_hi(vr[3 * (G)][k]), oa[2 * k + 1]); \
        oa[2 * k] = fmaf(pb, bf_lo(vr[3 * (G) + 1][k]), oa[2 * k]); oa[2 * k + 1] = fmaf(pb, bf_hi(vr[3 * (G) + 1][k]), oa[2 * k + 1]); \
        oc[2 * k] = fmaf(pc, bf_lo(vr[3 * (G) + 2][k]), oc[2 * k]); oc[2 * k + 1] = fmaf(pc, bf_hi(vr[3 * (G) + 2][k]), oc[2 * k + 1]); } \
      SB(); }
      PV_G(0) PV_G(1)
#undef PV_G
#pragma unroll
      for (int j = 0; j < 8; ++j) { PIN(oa[j]); PIN(oc[j]); }
      if (!more) break;
      task = 128 + (__builtin_amdgcn_readfirstlane(claim) & 0xffffff);
      if (task >= 255) l_f[F_EMPTY] = 1u;
      if (task >= 256) break;
    }
    const float lw = wave_sum(lrun) * 0.25f;
    float a4[4], a2[2];
#pragma unroll
    for (int j = 0; j < 4; ++j) a4[j] = swap32_add(oa[j], oa[j + 4]);
#pragma unroll
    for (int j = 0; j < 2; ++j) a2[j] = swap16_add(a4[j], a4[j + 2]);
    const float cv = treduce8(oc, bit3);
    float* lo = l_o + w * 192;
    *(LAS f2*)(lo + 8 * (lane & 15) + 2 * (lane >> 4)) = f2{a2[0], a2[1]};
    lo[128 + 8 * c8 + rg] = cv;
    if (lane == 0) *(LAS f2*)(l_ml + 2 * w) = f2{mrun, lw};
    LDS_FENCE();
    l_f[F_DONE + w] = 1u;
  }
  STAMP(4);
  if (w >= 8 || ABL) { STAMP_FLUSH(); return; }
  if (w >= 1 && w < 4) {
    const int d = (w - 1) * 64 + lane;
    gu64* dst = ws + WS_OP + (size_t)((h * 3 + w - 1) * 8 + s) * 64 + lane;
    wait_flags(l_f + F_DONE, 16, lane, 0);
    STAMP(5);
    const f2 ml = *(const LAS f2*)(l_ml + 2 * (lane & 15));
    float ov[16];
#pragma unroll
    for (int i = 0; i < 16; ++i) ov[i] = l_o[i * 192 + d];
    float M = ml.x;
    M = fmaxf(M, dpp<XOR1>(M)); M = fmaxf(M, dpp<XOR2>(M)); M = fmaxf(M, dpp<XOR7>(M)); M = fmaxf(M, dpp<ROR8>(M));
    const float sc = __builtin_amdgcn_exp2f(ml.x - M);
    float o = 0.f, o2 = 0.f;
#define CMB(i) o = fmaf(dpp<BCAST + (i)>(sc), ov[i], o); o2 = fmaf(dpp<BCAST + (i) + 1>(sc), ov[(i) + 1], o2);
    CMB(0) CMB(2) CMB(4) CMB(6) CMB(8) CMB(10) CMB(12) CMB(14)
#undef CMB
    put_f(dst, thi, tlo, o + o2);
    if (w == 1) {
      float L = sc * ml.y;
      L += dpp<XOR1>(L); L += dpp<XOR2>(L); L += dpp<XOR7>(L); L += dpp<ROR8>(L);
      const unsigned bits = lane == 0 ? __float_as_uint(M) : lane == 1 ? (__float_as_uint(M) << 24) : __float_as_uint(L);
      if (lane < 3) put_bits(ws + WS_ST + (size_t)b * 4 + lane, thi, tlo, bits);
    }
    l_f[F_PUB + w - 1] = 1u;
    STAMP(6);
    STAMP_FLUSH();
    return;
  }
  const int j5 = w >= 4 ? w - 4 : 4;
  if (b < 240) {
    float y;
    if (w == 0) {
      const gu64* gp = ws + WS_GP + (size_t)rgp * 1280 + lane;
      u64 g[20];
      for (unsigned spin = 0; spin < SPIN_CAP; ++spin) {
        bool ok = true;
#pragma unroll
        for (int k = 0; k < 20; ++k) g[k] = get_g(gp + k * 64);
#pragma unroll
        for (int k = 0; k < 20; ++k) ok = ok & g_ok(g[k], thi, tlo);
        if (__all(ok)) break;
        __builtin_amdgcn_s_sleep(8);
      }
      float gt = 0.f, up = 0.f;
#pragma unroll
      for (int k = 0; k < 10; ++k) { gt += g_val(g[2 * k]); up += g_val(g[2 * k + 1]); }
      gt = rbf(gt); up = rbf(up);
      y = rbf(rbf(gt / (1.0f + __expf(-gt))) * up);
    }
    const unsigned char* wb = (w >= 4 ? wo + (size_t)(256 * rgp + 64 * j5) * 1280 : w2 + (size_t)(64 * rgp) * 1280) + cc * 128;
    const __amdgpu_buffer_rsrc_t rw = __builtin_amdgcn_make_buffer_rsrc((void*)wb, 0, 63 * 1280 + 128, 0x00020000);
    wait_flags(l_f + F_EMPTY, 1, lane, 1);
    __builtin_amdgcn_s_setprio(0);
    v4u wr[8];
#pragma unroll
    for (int i = 0; i < 8; ++i) wr[i] = ld_nt(rw, rg * 8 * 1280 + c8 * 16, i * 1280);
    SB();
    __builtin_amdgcn_s_setprio(1);
    STAMP(5);
    float wf[64];
    if (w >= 4) {
      const int beta = 4 * rgp + j5, hd = beta / 3;
      const gu64* op = ws + WS_OP + (size_t)beta * 512 + lane;
      const gu64* sp = ws + WS_ST + (size_t)hd * 32 + (lane & 28) + min(lane & 3, 2);
      wait_flags(l_f + F_PUB, 3, lane, 1);
      u64 g[8], gs;
#pragma unroll
      for (int p = 0; p < 8; ++p) g[p] = get_g(op + p * 64);
      gs = get_g(sp);
      SB();
#pragma unroll
      for (int i = 0; i < 8; ++i)
#pragma unroll
        for (int k = 0; k < 4; ++k) { wf[8 * i + 2 * k] = bf_lo(wr[i][k]); wf[8 * i + 2 * k + 1] = bf_hi(wr[i][k]); PIN(wf[8 * i + 2 * k]); PIN(wf[8 * i + 2 * k + 1]); }
      SB();
      unsigned done = 0;
      float cfs[8];
      for (unsigned spin = 0; spin < SPIN_CAP; ++spin) {
        if (!((done >> 8) & 1)) {
          if (__all(g_ok(gs, thi, tlo))) {
            done |= 256u;
            const unsigned lo = (unsigned)gs;
            const unsigned mh = __builtin_amdgcn_mov_dpp(lo, 0x00, 0xf, 0xf, true), ml = __builtin_amdgcn_mov_dpp(lo, 0x55, 0xf, 0xf, true), ll = __builtin_amdgcn_mov_dpp(lo, 0xAA, 0xf, 0xf, true);
            const float Mp = __uint_as_float((mh << 8) | ((ml >> 16) & 0xffu)), Lp = __uint_as_float(ll << 8);
            const float M = wave_max(Mp);
            float cf = __builtin_amdgcn_exp2f(Mp - M);
            float L = cf * Lp;
            L += dpp<XOR7>(L); L += dpp<ROR8>(L); L = swap16_add(L, L);
            cf = cf / L;
#pragma unroll
            for (int p = 0; p < 8; ++p) cfs[p] = __builtin_bit_cast(float, __builtin_amdgcn_readlane(__builtin_bit_cast(int, cf), 4 * p));
          }
        }
#pragma unroll
        for (int p = 0; p < 8; ++p) if (!((done >> p) & 1)) { if (__all(g_ok(g[p], thi, tlo))) done |= 1u << p; }
        if (done == 511u) break;
#pragma unroll
        for (int p = 0; p < 8; ++p) if (!((done >> p) & 1)) g[p] = get_g(op + p * 64);
        if (!((done >> 8) & 1)) gs = get_g(sp);
      }
      STAMP(6);
      float o = 0.f;
#pragma unroll
      for (int p = 0; p < 8; ++p) o = fmaf(cfs[p], g_val(g[p]), o);
      y = o;
      y = rbf(y);
    } else {
#pragma unroll
      for (int i = 0; i < 8; ++i)
#pragma unroll
        for (int k = 0; k < 4; ++k) { wf[8 * i + 2 * k] = bf_lo(wr[i][k]); wf[8 * i + 2 * k + 1] = bf_hi(wr[i][k]); }
    }
    float acc[8];
#pragma unroll
    for (int j = 0; j < 8; ++j) acc[j] = 0.f;
#define GV(i) { const float y0 = dpp<BCAST + (i)>(y), y1 = dpp<BCAST + 8 + (i)>(y); const float yi = bit3 ? y1 : y0; \
    _Pragma("unroll") for (int k = 0; k < 8; ++k) acc[k] = fmaf(yi, wf[8 * (i) + k], acc[k]); }
    GV(0) GV(1) GV(2) GV(3) GV(4) GV(5) GV(6) GV(7)
#undef GV
    l_gv[j5 * 64 + 8 * c8 + rg] = treduce8(acc, bit3);
    LDS_FENCE();
    unsigned old = 0;
    if (lane == 0) old = __hip_atomic_fetch_add((LAS unsigned*)(l_f + F_CNT), 1u, __ATOMIC_RELAXED, __HIP_MEMORY_SCOPE_WORKGROUP);
    if (__builtin_amdgcn_readfirstlane(old) == 4u) {
      asm volatile("" ::: "memory");
      const float t = ((l_gv[lane] + l_gv[64 + lane]) + (l_gv[128 + lane] + l_gv[192 + lane])) + l_gv[256 + lane];
      put_f(ws + WS_FP + ((size_t)(cc * 8 + (lane >> 3)) * 24 + rgp) * FP_PAD + (lane & 7), thi, tlo, t);
    }
    STAMP(7);
  } else {
    const int r = (b - 240) * 5 + j5;
    const float xres = __uint_as_float((unsigned)xg[r * 8 + c8] << 16);
    gu64* fp = ws + WS_FP + ((size_t)r * 24 + rg) * FP_PAD + c8;
    wait_flags(l_f + F_PUB, 3, lane, 1);
    STAMP(5);
    u64 g[3] = {0, 0, 0};
    for (unsigned spin = 0; spin < SPIN_CAP; ++spin) {
      bool ok = true;
#pragma unroll
      for (int k = 0; k < 3; ++k) if (spin == 0 || !g_ok(g[k], thi, tlo)) g[k] = get_rmw(fp + k * 8 * FP_PAD);
#pragma unroll
      for (int k = 0; k < 3; ++k) ok = ok & g_ok(g[k], thi, tlo);
      if (__all(ok)) break;
    }
    float t = (g_val(g[0]) + g_val(g[1])) + g_val(g[2]);
    t += dpp<ROR8>(t);
    t = swap16_add(t, t);
    t = swap32_add(t, t);
    t += xres;
    if (lane < 8) out[r * 8 + lane] = __builtin_bit_cast(u16, (__bf16)t);
    if (r == 0 && lane == 0) __hip_atomic_store(ws + WS_EPOCH, ((u64)e1hi << 32) | e1lo, RLX_AGENT);
    STAMP(8);
  }
  STAMP_FLUSH();
}

extern "C" void kernel_launch(void* const* d_in, const int* in_sizes, int n_in, void* d_out, int out_size, void* d_ws, size_t ws_size,
                              hipStream_t stream) {
  (void)in_sizes; (void)n_in; (void)out_size; (void)ws_size;
  fused_decode<<<dim3(NWG), dim3(NTHR), 0, stream>>>((const u16*)d_in[0], (const unsigned char*)d_in[1], (const unsigned char*)d_in[2],
                                                      (const unsigned char*)d_in[3], (const unsigned char*)d_in[4], (const unsigned char*)d_in[6],
                                                      (const unsigned char*)d_in[7], (const unsigned char*)d_in[8], (u16*)d_out, (u64*)d_ws);
}
```

```cpp
#include <hip/hip_runtime.h>
#include <hip/hip_bf16.h>

typedef unsigned v4u __attribute__((ext_vector_type(4)));
typedef float f2 __attribute__((ext_vector_type(2)));
typedef unsigned long long u64;
typedef unsigned short u16;
typedef __attribute__((ext_vector_type(2))) __bf16 bf16x2;
#define GAS __attribute__((address_space(1)))
#define LAS __attribute__((address_space(3)))
typedef GAS u64 gu64;
#define RLX_AGENT __ATOMIC_RELAXED, __HIP_MEMORY_SCOPE_AGENT

#define MIMIC 1

constexpr int DM = 640, NH = 32, SK = 32768, DH = 192, WIN = 4096, FF = 1536;
constexpr int NWG = 256, NTHR = 512;
constexpr int ROWB = DH * 2;
constexpr size_t WS_EPOCH = 0;
constexpr size_t WS_QP = 64;
constexpr size_t WS_OP = WS_QP + (size_t)NWG * 320;
constexpr size_t WS_FP = WS_OP + (size_t)NWG * 256;
constexpr size_t WS_END = WS_FP + (size_t)80 * 2048;
constexpr unsigned SPIN_CAP = 1u << 18;
constexpr float LOG2E = 1.4426950408889634f;

__device__ __forceinline__ float bf_lo(unsigned d) { return __uint_as_float(d << 16); }
__device__ __forceinline__ float bf_hi(unsigned d) { return __uint_as_float(d & 0xffff0000u); }
__device__ __forceinline__ unsigned pk_bf16(float lo, float hi) {
  bf16x2 v = {(__bf16)lo, (__bf16)hi};
  return __builtin_bit_cast(unsigned, v);
}
__device__ __forceinline__ float rbf(float x) { return (float)(__bf16)x; }
__device__ __forceinline__ float dot2(unsigned a, unsigned b, float acc) {
  return __builtin_amdgcn_fdot2_f32_bf16(__builtin_bit_cast(bf16x2, a), __builtin_bit_cast(bf16x2, b), acc, false);
}
template <int CTRL> __device__ __forceinline__ float dpp(float x) {
  return __builtin_bit_cast(float, __builtin_amdgcn_mov_dpp(__builtin_bit_cast(int, x), CTRL, 0xf, 0xf, true));
}
constexpr int XOR1 = 0xB1, XOR2 = 0x4E, XOR7 = 0x141, ROR8 = 0x128, BCAST = 0x150;
__device__ __forceinline__ float swap32_add(float a, float b) {
  auto r = __builtin_amdgcn_permlane32_swap(__float_as_uint(a), __float_as_uint(b), false, false);
  return __uint_as_float(r[0]) + __uint_as_float(r[1]);
}
__device__ __forceinline__ float swap16_add(float a, float b) {
  auto r = __builtin_amdgcn_permlane16_swap(__float_as_uint(a), __float_as_uint(b), false, false);
  return __uint_as_float(r[0]) + __uint_as_float(r[1]);
}
__device__ __forceinline__ float wave_sum(float x) {
  x += dpp<XOR1>(x); x += dpp<XOR2>(x); x += dpp<XOR7>(x); x += dpp<ROR8>(x);
  x = swap16_add(x, x); x = swap32_add(x, x);
  return x;
}
__device__ __forceinline__ float wave_max(float x) {
  x = fmaxf(x, dpp<XOR1>(x)); x = fmaxf(x, dpp<XOR2>(x)); x = fmaxf(x, dpp<XOR7>(x)); x = fmaxf(x, dpp<ROR8>(x));
  auto r = __builtin_amdgcn_permlane16_swap(__float_as_uint(x), __float_as_uint(x), false, false);
  x = fmaxf(__uint_as_float(r[0]), __uint_as_float(r[1]));
  auto t = __builtin_amdgcn_permlane32_swap(__float_as_uint(x), __float_as_uint(x), false, false);
  return fmaxf(__uint_as_float(t[0]), __uint_as_float(t[1]));
}
__device__ __forceinline__ void put_bits(gu64* g, unsigned thi, unsigned tlo, unsigned bits) {
  const unsigned lo = __builtin_amdgcn_alignbit(tlo, bits, 8);
  __hip_atomic_store(g, ((u64)thi << 32) | lo, RLX_AGENT);
}
__device__ __forceinline__ void put_f(gu64* g, unsigned thi, unsigned tlo, float v) { put_bits(g, thi, tlo, __float_as_uint(v)); }
__device__ __forceinline__ u64 get_g(const gu64* g) { return __hip_atomic_load(const_cast<gu64*>(g), RLX_AGENT); }
__device__ __forceinline__ bool g_ok(u64 x, unsigned thi, unsigned tlo) { return ((unsigned)(x >> 32) == thi) & (((unsigned)x >> 24) == tlo); }
__device__ __forceinline__ float g_val(u64 x) { return __uint_as_float((unsigned)x << 8); }
__device__ __forceinline__ v4u ld_nt(__amdgpu_buffer_rsrc_t r, int voff, int soff) { return __builtin_amdgcn_raw_buffer_load_b128(r, voff, soff, 2); }
#define SB() __builtin_amdgcn_sched_barrier(0)
#define PIN(x) asm volatile("" : "+v"(x))

#define STAMP(i) do { } while (0)
#define STAMP_FLUSH() do { } while (0)
constexpr int VPRE = 4;

__global__ void __launch_bounds__(NTHR, 2)
fused_decode(const u16* __restrict__ xg, const unsigned char* __restrict__ Kc, const unsigned char* __restrict__ Vc,
             const unsigned char* __restrict__ wq, const unsigned char* __restrict__ wo, const unsigned char* __restrict__ w1,
             const unsigned char* __restrict__ w3, const unsigned char* __restrict__ w2, u16* __restrict__ out, u64* __restrict__ wsp) {
  __shared__ __attribute__((aligned(16))) float lds[8 * 192 + 96 + 32 + 16 + 64 + 16];
  float* l_o = lds;
  unsigned* l_q = (unsigned*)(lds + 1536);
  float* l_y = lds + 1536 + 96;
  float* l_ml = lds + 1536 + 128;
  float* l_red = lds + 1536 + 144;
  volatile LAS unsigned* l_flag = (volatile LAS unsigned*)(lds + 1536 + 208);

  const int b = blockIdx.x, tid = threadIdx.x, lane = tid & 63, w = __builtin_amdgcn_readfirstlane(tid >> 6);
  const int h = b >> 3, s = b & 7;
  gu64* ws = (gu64*)wsp;
  STAMP(0);
  __builtin_amdgcn_s_setprio(3);
  int vz = 0; PIN(vz);
  const u64 e0 = get_g(ws + WS_EPOCH + vz);
  if (tid == 0) *l_flag = 0u;

  const size_t kvbase = ((size_t)h * SK + (SK - WIN) + s * 512 + w * 64) * ROWB;
  const __amdgpu_buffer_rsrc_t rk = __builtin_amdgcn_make_buffer_rsrc((void*)(Kc + kvbase), 0, 64 * ROWB, 0x00020000);
  const __amdgpu_buffer_rsrc_t rv = __builtin_amdgcn_make_buffer_rsrc((void*)(Vc + kvbase), 0, 64 * ROWB, 0x00020000);
  const int bit3 = (lane >> 3) & 1;
  const int offA = (lane >> 4) * ROWB + (lane & 15) * 16;
  const int offC = ((lane >> 4) + 4 * bit3) * ROWB + 256 + (lane & 7) * 16;
  const float xres = __uint_as_float((unsigned)xg[(b < 80 ? b : 0) * 8 + (lane & 7)] << 16);

  unsigned thi, tlo;
  u64 e1;
  v4u kr[24];
  if (w < 5) {
    const int rg = lane >> 3, c = lane & 7;
    unsigned xa[5], xr[5];
    const unsigned* x32 = (const unsigned*)xg;
#pragma unroll
    for (int i = 0; i < 5; ++i) xa[i] = x32[lane + 64 * i];
#pragma unroll
    for (int i = 0; i < 5; ++i) xr[i] = x32[40 * s + 5 * rg + i];
    const unsigned char* wb = w < 3 ? wq + (size_t)h * 384 + w * 128 : (w == 3 ? w1 : w3) + (size_t)h * 96;
    const int stride = w < 3 ? 12288 : 3072;
    const int seg = w < 3 ? 128 : 96;
    const __amdgpu_buffer_rsrc_t rw = __builtin_amdgcn_make_buffer_rsrc((void*)(wb + (size_t)(80 * s) * stride), 0, 79 * stride + seg, 0x00020000);
    const int woff = (c * 16 < seg) ? rg * 10 * stride + c * 16 : 0x7fffff00;
    v4u wr[10];
#pragma unroll
    for (int i = 0; i < 10; ++i) wr[i] = ld_nt(rw, woff, i * stride);
    SB();
    asm volatile("s_waitcnt lgkmcnt(0)\n\ts_barrier" ::: "memory");
    SB();
    float sm = 0.f;
#pragma unroll
    for (int i = 0; i < 5; ++i) sm += bf_lo(xa[i]) + bf_hi(xa[i]);
    const float mean = wave_sum(sm) * (1.0f / DM);
    float sq = 0.f;
#pragma unroll
    for (int i = 0; i < 5; ++i) { const float a = bf_lo(xa[i]) - mean, c2 = bf_hi(xa[i]) - mean; sq += a * a + c2 * c2; }
    const float var = wave_sum(sq) * (1.0f / DM);
    float xn[10];
    const float mb = rbf(mean), den = rbf(sqrtf(rbf(rbf(var) + 1e-5f)));
#pragma unroll
    for (int i = 0; i < 5; ++i) { xn[2 * i] = rbf(rbf(bf_lo(xr[i]) - mb) / den); xn[2 * i + 1] = rbf(rbf(bf_hi(xr[i]) - mb) / den); }
    e1 = e0 * 6364136223846793005ull + 1442695040888963407ull;
    thi = (unsigned)(e1 >> 8); tlo = (unsigned)e1 & 0xffu;
    float acc[8];
#pragma unroll
    for (int j = 0; j < 8; ++j) acc[j] = 0.f;
#pragma unroll
    for (int i = 0; i < 10; ++i)
#pragma unroll
      for (int k = 0; k < 4; ++k) { acc[2 * k] = fmaf(xn[i], bf_lo(wr[i][k]), acc[2 * k]); acc[2 * k + 1] = fmaf(xn[i], bf_hi(wr[i][k]), acc[2 * k + 1]); }
    float a4[4], a2[2];
#pragma unroll
    for (int j = 0; j < 4; ++j) a4[j] = swap32_add(acc[j], acc[j + 4]);
#pragma unroll
    for (int j = 0; j < 2; ++j) a2[j] = swap16_add(a4[j], a4[j + 2]);
    const float t0 = a2[0] + dpp<ROR8>(a2[0]), t1 = a2[1] + dpp<ROR8>(a2[1]);
    const float tot = bit3 ? t1 : t0;
    put_f(ws + WS_QP + (size_t)b * 320 + w * 64 + 8 * c + rg, thi, tlo, tot);
    SB();
    STAMP(1);
    if (w == 0) {
      const gu64* qp = ws + WS_QP + (size_t)h * 8 * 320;
      u64 g[24], gg, gu;
      const int fo = (lane & 7) * 320 + 192 + 6 * s + min(lane >> 3, 5);
      unsigned done = 0;
      for (unsigned spin = 0; spin < SPIN_CAP; ++spin) {
#pragma unroll
        for (int p = 0; p < 8; ++p)
          if (!((done >> p) & 1)) {
#pragma unroll
            for (int j = 0; j < 3; ++j) g[p * 3 + j] = get_g(qp + p * 320 + j * 64 + lane);
          }
        if (!((done >> 8) & 1)) { gg = get_g(qp + fo); gu = get_g(qp + fo + 64); }
#pragma unroll
        for (int p = 0; p < 8; ++p)
          if (!((done >> p) & 1)) {
            const bool ok = g_ok(g[p * 3], thi, tlo) & g_ok(g[p * 3 + 1], thi, tlo) & g_ok(g[p * 3 + 2], thi, tlo);
            if (__all(ok)) done |= 1u << p;
          }
        if (!((done >> 8) & 1)) { if (__all(g_ok(gg, thi, tlo) & g_ok(gu, thi, tlo))) done |= 256u; }
        if (done == 511u) break;
        __builtin_amdgcn_s_sleep(4);
      }
      float qv[3];
#pragma unroll
      for (int j = 0; j < 3; ++j) {
        float t = 0.f;
#pragma unroll
        for (int p = 0; p < 8; ++p) t += g_val(g[p * 3 + j]);
        qv[j] = t;
      }
#pragma unroll
      for (int j = 0; j < 3; ++j) ((LAS __bf16*)l_q)[j * 64 + lane] = (__bf16)qv[j];
      float gt = g_val(gg), up = g_val(gu);
      gt += dpp<XOR1>(gt); gt += dpp<XOR2>(gt); gt += dpp<XOR7>(gt);
      up += dpp<XOR1>(up); up += dpp<XOR2>(up); up += dpp<XOR7>(up);
      gt = rbf(gt); up = rbf(up);
      const float hv = rbf(rbf(gt / (1.0f + __expf(-gt))) * up);
      if ((lane & 7) == 0 && lane < 48) l_y[24 + (lane >> 3)] = hv;
      if (lane >= 62) l_y[30 + (lane & 1)] = 0.f;
      asm volatile("s_waitcnt lgkmcnt(0)" ::: "memory");
      *l_flag = 1u;
      STAMP(2);
    }
  } else {
    SB();
  }
#pragma unroll
  for (int g = 0; g < 8; ++g) {
    kr[3 * g] = ld_nt(rk, offA, g * 3072);
    kr[3 * g + 1] = ld_nt(rk, offA, g * 3072 + 1536);
    kr[3 * g + 2] = ld_nt(rk, offC, g * 3072);
    SB();
    if (g == 0 && w >= 5) { asm volatile("s_barrier" ::: "memory"); SB(); }
  }
  SB();
  __builtin_amdgcn_s_setprio(1);
  STAMP(3);
  if (w >= 5) {
    e1 = e0 * 6364136223846793005ull + 1442695040888963407ull;
    thi = (unsigned)(e1 >> 8); tlo = (unsigned)e1 & 0xffu;
  }
  v4u vr[24];
#pragma unroll
  for (int g = 0; g < VPRE; ++g) {
    vr[3 * g] = ld_nt(rv, offA, g * 3072);
    vr[3 * g + 1] = ld_nt(rv, offA, g * 3072 + 1536);
    vr[3 * g + 2] = ld_nt(rv, offC, g * 3072);
    SB();
  }
  SB();
  STAMP(4);
  while (*l_flag == 0u) __builtin_amdgcn_s_sleep(2);
  SB();
  STAMP(5);
  const v4u qa = *(const LAS v4u*)(l_q + (lane & 15) * 4);
  const v4u qc = *(const LAS v4u*)(l_q + 64 + (lane & 7) * 4);
  float xs[8];
#pragma unroll
  for (int g = 0; g < 8; ++g) {
    float sa = 0.f, sb = 0.f, sc = 0.f;
#pragma unroll
    for (int k = 0; k < 4; ++k) { sa = dot2(kr[3 * g][k], qa[k], sa); sb = dot2(kr[3 * g + 1][k], qa[k], sb); sc = dot2(kr[3 * g + 2][k], qc[k], sc); }
    const float keep = bit3 ? sb : sa, send = bit3 ? sa : sb;
    xs[g] = keep + sc + dpp<ROR8>(send);
  }
  float x4[4], x2[2];
#pragma unroll
  for (int j = 0; j < 4; ++j) { const float keep = (lane & 4) ? xs[j + 4] : xs[j], send = (lane & 4) ? xs[j] : xs[j + 4]; x4[j] = keep + dpp<XOR7>(send); }
#pragma unroll
  for (int j = 0; j < 2; ++j) { const float keep = (lane & 2) ? x4[j + 2] : x4[j], send = (lane & 2) ? x4[j] : x4[j + 2]; x2[j] = keep + dpp<XOR2>(send); }
  float sco = ((lane & 1) ? x2[1] : x2[0]) + dpp<XOR1>((lane & 1) ? x2[0] : x2[1]);
  SB();
  __builtin_amdgcn_s_setprio(0);
  STAMP(6);
#pragma unroll
  for (int g = VPRE; g < 8; ++g) {
    vr[3 * g] = ld_nt(rv, offA, g * 3072);
    vr[3 * g + 1] = ld_nt(rv, offA, g * 3072 + 1536);
    vr[3 * g + 2] = ld_nt(rv, offC, g * 3072);
    SB();
  }
  unsigned wd[30];
  {
    const __amdgpu_buffer_rsrc_t ro = __builtin_amdgcn_make_buffer_rsrc((void*)(wo + (size_t)(192 * h + 24 * s) * 1280), 0, 24 * 1280, 0x00020000);
    const __amdgpu_buffer_rsrc_t r2 = __builtin_amdgcn_make_buffer_rsrc((void*)(w2 + (size_t)(48 * h + 6 * s) * 1280), 0, 6 * 1280, 0x00020000);
    const int po = tid < 320 ? tid * 4 : 0x7fffff00;
#pragma unroll
    for (int i = 0; i < 24; ++i) wd[i] = __builtin_amdgcn_raw_buffer_load_b32(ro, po, i * 1280, 2);
#pragma unroll
    for (int i = 0; i < 6; ++i) wd[24 + i] = __builtin_amdgcn_raw_buffer_load_b32(r2, po, i * 1280, 2);
  }
  SB();
  STAMP(7);
  sco = rbf(rbf(sco) * 0.07216878364870322f) * LOG2E;
  const float mw = wave_max(sco);
  const float p = __builtin_amdgcn_exp2f(sco - mw);
  const float lw = wave_sum(p);
  if (lane == 0) { l_ml[2 * w] = mw; l_ml[2 * w + 1] = lw; }
  float oa[8], oc[8];
#pragma unroll
  for (int j = 0; j < 8; ++j) { oa[j] = 0.f; oc[j] = 0.f; }
#define PV_G(G) { \
    const float pa = dpp<BCAST + (G)>(p), pb = dpp<BCAST + 8 + (G)>(p); const float pc = bit3 ? pb : pa; \
    _Pragma("unroll") for (int k = 0; k < 4; ++k) { \
      oa[2 * k] = fmaf(pa, bf_lo(vr[3 * (G)][k]), oa[2 * k]); oa[2 * k + 1] = fmaf(pa, bf_hi(vr[3 * (G)][k]), oa[2 * k + 1]); \
      oa[2 * k] = fmaf(pb, bf_lo(vr[3 * (G) + 1][k]), oa[2 * k]); oa[2 * k + 1] = fmaf(pb, bf_hi(vr[3 * (G) + 1][k]), oa[2 * k + 1]); \
      oc[2 * k] = fmaf(pc, bf_lo(vr[3 * (G) + 2][k]), oc[2 * k]); oc[2 * k + 1] = fmaf(pc, bf_hi(vr[3 * (G) + 2][k]), oc[2 * k + 1]); } \
    SB(); }
  PV_G(0) PV_G(1) PV_G(2) PV_G(3) PV_G(4) PV_G(5) PV_G(6) PV_G(7)
#undef PV_G
  {
    float a4[4], c4[4], a2[2], c2[2];
#pragma unroll
    for (int j = 0; j < 4; ++j) { a4[j] = swap32_add(oa[j], oa[j + 4]); c4[j] = swap32_add(oc[j], oc[j + 4]); }
#pragma unroll
    for (int j = 0; j < 2; ++j) { a2[j] = swap16_add(a4[j], a4[j + 2]); c2[j] = swap16_add(c4[j], c4[j + 2]); }
    const float cc = (bit3 ? c2[1] : c2[0]) + dpp<ROR8>(bit3 ? c2[0] : c2[1]);
    float* lo = l_o + w * 192;
    *(f2*)(lo + 8 * (lane & 15) + 2 * (lane >> 4)) = f2{a2[0], a2[1]};
    lo[128 + 8 * (lane & 7) + (lane >> 3)] = cc;
  }
  STAMP(8);
  __syncthreads();
  if (tid < 256) {
    const int cs = tid >> 5, j = tid & 31;
    float M = l_ml[0];
#pragma unroll
    for (int i = 1; i < 8; ++i) M = fmaxf(M, l_ml[2 * i]);
    float L = 0.f, o = 0.f;
    const int d = 24 * cs + min(j, 23);
#pragma unroll
    for (int i = 0; i < 8; ++i) { const float sc = __builtin_amdgcn_exp2f(l_ml[2 * i] - M); L = fmaf(sc, l_ml[2 * i + 1], L); o = fmaf(sc, l_o[i * 192 + d], o); }
    const unsigned bits = j < 24 ? __float_as_uint(o) : j == 24 ? __float_as_uint(M) : j == 25 ? (__float_as_uint(M) << 24) : __float_as_uint(L);
    if (j < 27) put_bits(ws + WS_OP + ((size_t)(h * 8 + cs) * 8 + s) * 32 + j, thi, tlo, bits);
  }
  float wl[30], wh[30];
#pragma unroll
  for (int i = 0; i < 30; ++i) { wl[i] = bf_lo(wd[i]); wh[i] = bf_hi(wd[i]); PIN(wl[i]); PIN(wh[i]); }
  if (w == 7) {
    const gu64* op = ws + WS_OP + (size_t)b * 256;
    const int j = lane & 31;
    u64 g[4];
    for (unsigned spin = 0; spin < SPIN_CAP; ++spin) {
      bool ok = true;
#pragma unroll
      for (int k = 0; k < 4; ++k) g[k] = get_g(op + k * 64 + (lane & 32) + min(j, 26));
#pragma unroll
      for (int k = 0; k < 4; ++k) ok = ok & g_ok(g[k], thi, tlo);
      if (__all(ok)) break;
      __builtin_amdgcn_s_sleep(1);
    }
    float Mp[8], Lp[8];
#pragma unroll
    for (int k = 0; k < 4; ++k)
#pragma unroll
      for (int hf = 0; hf < 2; ++hf) {
        const unsigned lo = (unsigned)g[k];
        const unsigned mh = __builtin_amdgcn_readlane(lo, 32 * hf + 24), ml = __builtin_amdgcn_readlane(lo, 32 * hf + 25), ll = __builtin_amdgcn_readlane(lo, 32 * hf + 26);
        Mp[2 * k + hf] = __uint_as_float((mh << 8) | ((ml >> 16) & 0xffu));
        Lp[2 * k + hf] = __uint_as_float(ll << 8);
      }
    float M = Mp[0];
#pragma unroll
    for (int i = 1; i < 8; ++i) M = fmaxf(M, Mp[i]);
    float L = 0.f, cf[8];
#pragma unroll
    for (int i = 0; i < 8; ++i) { cf[i] = __builtin_amdgcn_exp2f(Mp[i] - M); L = fmaf(cf[i], Lp[i], L); }
    float o = 0.f;
#pragma unroll
    for (int k = 0; k < 4; ++k) { float c0 = cf[2 * k], c1 = cf[2 * k + 1]; PIN(c0); PIN(c1); o = fmaf(lane < 32 ? c0 : c1, g_val(g[k]), o); }
    o = swap32_add(o, o) / L;
    o = rbf(o);
    if (lane < 24) l_y[lane] = o;
  }
  STAMP(9);
  __syncthreads();
  if (w < 5) {
    float a0 = 0.f, a1 = 0.f;
#pragma unroll
    for (int i = 0; i < 30; ++i) { const float y = l_y[i]; a0 = fmaf(y, wl[i], a0); a1 = fmaf(y, wh[i], a1); }
    gu64* fp = ws + WS_FP + ((size_t)(tid >> 2) * 256 + b) * 8 + (tid & 3) * 2;
    put_f(fp, thi, tlo, a0);
    put_f(fp + 1, thi, tlo, a1);
  }
  STAMP(10);
  if (b >= 80) { STAMP_FLUSH(); return; }
  {
    const gu64* fp = ws + WS_FP + (size_t)b * 2048 + tid;
    u64 g[4];
    for (unsigned spin = 0; spin < SPIN_CAP; ++spin) {
      bool ok = true;
#pragma unroll
      for (int k = 0; k < 4; ++k) g[k] = get_g(fp + k * 512);
#pragma unroll
      for (int k = 0; k < 4; ++k) ok = ok & g_ok(g[k], thi, tlo);
      if (__all(ok)) break;
      __builtin_amdgcn_s_sleep(1);
    }
    float t = (g_val(g[0]) + g_val(g[1])) + (g_val(g[2]) + g_val(g[3]));
    t += dpp<ROR8>(t);
    t = swap16_add(t, t);
    t = swap32_add(t, t);
    if (lane < 8) l_red[w * 8 + lane] = t;
    __syncthreads();
    if (tid < 8) {
      float r = 0.f;
#pragma unroll
      for (int i = 0; i < 8; ++i) r += l_red[i * 8 + tid];
      r += xres;
      out[b * 8 + tid] = __builtin_bit_cast(u16, (__bf16)r);
      if (b == 0 && tid == 0) __hip_atomic_store(ws + WS_EPOCH, e1, RLX_AGENT);
    }
    STAMP(11);
    STAMP_FLUSH();
  }
}

extern "C" void kernel_launch(void* const* d_in, const int* in_sizes, int n_in, void* d_out, int out_size, void* d_ws, size_t ws_size,
                              hipStream_t stream) {
  (void)in_sizes; (void)n_in; (void)out_size; (void)ws_size;
  fused_decode<<<dim3(NWG), dim3(NTHR), 0, stream>>>((const u16*)d_in[0], (const unsigned char*)d_in[1], (const unsigned char*)d_in[2],
                                                      (const unsigned char*)d_in[3], (const unsigned char*)d_in[4], (const unsigned char*)d_in[6],
                                                      (const unsigned char*)d_in[7], (const unsigned char*)d_in[8], (u16*)d_out, (u64*)d_ws);
}
```

```cpp
#include <hip/hip_runtime.h>
#include <hip/hip_bf16.h>

typedef unsigned v4u __attribute__((ext_vector_type(4)));
typedef float f2 __attribute__((ext_vector_type(2)));
typedef unsigned long long u64;
typedef unsigned short u16;
typedef __attribute__((ext_vector_type(2))) __bf16 bf16x2;
#define GAS __attribute__((address_space(1)))
#define LAS __attribute__((address_space(3)))
typedef GAS u64 gu64;
#define RLX_AGENT __ATOMIC_RELAXED, __HIP_MEMORY_SCOPE_AGENT

#define MIMIC 1
#define WO_LATE 0
#define SLEEP_LONG 4
#define SLEEP_SHORT 0
#define ABL 0
#define PW0 12
#define TLAST 0
#define TAU16 176
#define WOFF 0
#define GORG (TLAST != 0)
#define XMAP 1
#define PDEPTH 1
#define PGAP 4
#define FP_PAD 16

constexpr int DM = 640, SK = 32768, WIN = 4096;
constexpr int NWG = 256, NTHR = 1024;
constexpr int ROWB = 384;
constexpr size_t WS_EPOCH = 0;
constexpr size_t WS_QP = 64;
constexpr size_t WS_GP = WS_QP + (size_t)NWG * 192;
constexpr size_t WS_OP = WS_GP + (size_t)240 * 128;
constexpr size_t WS_ST = WS_OP + (size_t)NWG * 192;
constexpr size_t WS_FP = WS_ST + (size_t)NWG * 4;
constexpr size_t WS_TS = WS_FP + (size_t)80 * 24 * FP_PAD;
constexpr size_t WS_END = WS_TS + 32 * 16;
constexpr unsigned SPIN_CAP = 1u << 18;
constexpr float LOG2E = 1.4426950408889634f;
constexpr int OOB = 0x7fffff00;

__device__ __forceinline__ float bf_lo(unsigned d) { return __uint_as_float(d << 16); }
__device__ __forceinline__ float bf_hi(unsigned d) { return __uint_as_float(d & 0xffff0000u); }
__device__ __forceinline__ float rbf(float x) { return (float)(__bf16)x; }
__device__ __forceinline__ float dot2(unsigned a, unsigned b, float acc) {
  return __builtin_amdgcn_fdot2_f32_bf16(__builtin_bit_cast(bf16x2, a), __builtin_bit_cast(bf16x2, b), acc, false);
}
template <int CTRL> __device__ __forceinline__ float dpp(float x) {
  return __builtin_bit_cast(float, __builtin_amdgcn_mov_dpp(__builtin_bit_cast(int, x), CTRL, 0xf, 0xf, true));
}
constexpr int XOR1 = 0xB1, XOR2 = 0x4E, XOR7 = 0x141, ROR8 = 0x128, BCAST = 0x150;
__device__ __forceinline__ float swap32_add(float a, float b) {
  auto r = __builtin_amdgcn_permlane32_swap(__float_as_uint(a), __float_as_uint(b), false, false);
  return __uint_as_float(r[0]) + __uint_as_float(r[1]);
}
__device__ __forceinline__ float swap16_add(float a, float b) {
  auto r = __builtin_amdgcn_permlane16_swap(__float_as_uint(a), __float_as_uint(b), false, false);
  return __uint_as_float(r[0]) + __uint_as_float(r[1]);
}
__device__ __forceinline__ float wave_sum(float x) {
  x += dpp<XOR1>(x); x += dpp<XOR2>(x); x += dpp<XOR7>(x); x += dpp<ROR8>(x);
  x = swap16_add(x, x); x = swap32_add(x, x);
  return x;
}
__device__ __forceinline__ float wave_max(float x) {
  x = fmaxf(x, dpp<XOR1>(x)); x = fmaxf(x, dpp<XOR2>(x)); x = fmaxf(x, dpp<XOR7>(x)); x = fmaxf(x, dpp<ROR8>(x));
  auto r = __builtin_amdgcn_permlane16_swap(__float_as_uint(x), __float_as_uint(x), false, false);
  x = fmaxf(__uint_as_float(r[0]), __uint_as_float(r[1]));
  auto t = __builtin_amdgcn_permlane32_swap(__float_as_uint(x), __float_as_uint(x), false, false);
  return fmaxf(__uint_as_float(t[0]), __uint_as_float(t[1]));
}
__device__ __forceinline__ float treduce8(const float (&acc)[8], int bit3) {
  float a4[4], a2[2];
#pragma unroll
  for (int j = 0; j < 4; ++j) a4[j] = swap32_add(acc[j], acc[j + 4]);
#pragma unroll
  for (int j = 0; j < 2; ++j) a2[j] = swap16_add(a4[j], a4[j + 2]);
  return (bit3 ? a2[1] : a2[0]) + dpp<ROR8>(bit3 ? a2[0] : a2[1]);
}
__device__ __forceinline__ void put_bits(gu64* g, unsigned thi, unsigned tlo, unsigned bits) {
  const unsigned lo = __builtin_amdgcn_alignbit(tlo, bits, 8);
  __hip_atomic_store(g, ((u64)thi << 32) | lo, RLX_AGENT);
}
__device__ __forceinline__ void put_f(gu64* g, unsigned thi, unsigned tlo, float v) { put_bits(g, thi, tlo, __float_as_uint(v)); }
__device__ __forceinline__ u64 get_g(const gu64* g) { return __hip_atomic_load(const_cast<gu64*>(g), RLX_AGENT); }
__device__ __forceinline__ u64 get_rmw(gu64* g) { u64 z = 0; asm volatile("" : "+v"(z)); return __hip_atomic_fetch_or(g, z, RLX_AGENT); }
__device__ __forceinline__ bool g_ok(u64 x, unsigned thi, unsigned tlo) { return ((unsigned)(x >> 32) == thi) & (((unsigned)x >> 24) == tlo); }
__device__ __forceinline__ float g_val(u64 x) { return __uint_as_float((unsigned)x << 8); }
__device__ __forceinline__ u64 next_epoch(u64 e0) {
  const u64 e = ((u64)(unsigned)__builtin_amdgcn_readfirstlane((unsigned)(e0 >> 32)) << 32) | (unsigned)__builtin_amdgcn_readfirstlane((unsigned)e0);
  return e * 6364136223846793005ull + 1442695040888963407ull;
}
__device__ __forceinline__ v4u ld_nt(__amdgpu_buffer_rsrc_t r, int voff, int soff) { return __builtin_amdgcn_raw_buffer_load_b128(r, voff, soff, 2); }
#define SB() __builtin_amdgcn_sched_barrier(0)
#define PIN(x) asm volatile("" : "+v"(x))
#define LDS_ORDER() asm volatile("" ::: "memory")
#define LDS_FENCE() asm volatile("s_waitcnt lgkmcnt(0)" ::: "memory")

#define STAMP(i) do { } while (0)
#define STAMP_FLUSH() do { } while (0)

constexpr int F_DONE = 0, F_ST = 16, F_Q = 32, F_PUB = 36, F_X = 40, F_CNT = 41;

__device__ __forceinline__ void wait_flags(volatile LAS unsigned* f, int n, int lane, int sleep) {
  const int i = lane < n ? lane : 0;
  while (!__all(f[i] != 0u)) { if (sleep) __builtin_amdgcn_s_sleep(SLEEP_LONG); else __builtin_amdgcn_s_sleep(SLEEP_SHORT); }
  asm volatile("" ::: "memory");
}

__global__ void __launch_bounds__(NTHR, 4)
fused_decode(const u16* __restrict__ xg, const unsigned char* __restrict__ Kc, const unsigned char* __restrict__ Vc,
             const unsigned char* __restrict__ wq, const unsigned char* __restrict__ wo, const unsigned char* __restrict__ w1,
             const unsigned char* __restrict__ w3, const unsigned char* __restrict__ w2, u16* __restrict__ out, u64* __restrict__ wsp) {
  __shared__ __attribute__((aligned(16))) float lds[16 * 192 + 96 + 32 + 320 + 648 + 64 + 4];
  float* l_o = lds;
  unsigned* l_q = (unsigned*)(lds + 3072);
  float* l_ml = lds + 3072 + 96;
  float* l_gv = lds + 3072 + 128;
  float* l_xn = lds + 3072 + 448;
  volatile LAS unsigned* l_f = (volatile LAS unsigned*)(lds + 3072 + 1096);
  volatile LAS unsigned* l_tag = (volatile LAS unsigned*)(lds + 3072 + 1160);

  const int b = blockIdx.x, tid = threadIdx.x, lane = tid & 63, w = __builtin_amdgcn_readfirstlane(tid >> 6);
  const int h = b >> 3, s = b & 7;
  const int tile = XMAP ? s * 30 + h : b;
  const int rgp = tile / 10, cc = tile - rgp * 10;
  gu64* ws = (gu64*)wsp;
  STAMP(0);
  unsigned tstart = (unsigned)__builtin_amdgcn_s_memrealtime();
  const size_t kvbase = ((size_t)h * SK + (SK - WIN) + s * 512 + w * 32) * ROWB;
  const __amdgpu_buffer_rsrc_t rk = __builtin_amdgcn_make_buffer_rsrc((void*)(Kc + kvbase), 0, 32 * ROWB, 0x00020000);
  const __amdgpu_buffer_rsrc_t rv = __builtin_amdgcn_make_buffer_rsrc((void*)(Vc + kvbase), 0, 32 * ROWB, 0x00020000);
  const int bit3 = (lane >> 3) & 1, rg = lane >> 3, c8 = lane & 7;
  const int offA = (lane >> 4) * ROWB + (lane & 15) * 16;
  const int offC = ((lane >> 4) + 4 * bit3) * ROWB + 256 + c8 * 16;

  __builtin_amdgcn_s_setprio(1);
  if (w < 6) {
    if (w == 3) {
      int vz = 0; PIN(vz);
      const u64 e0 = get_g(ws + WS_EPOCH + vz);
      const v4u xa = *(const v4u*)(xg + lane * 8);
      const unsigned xb = *(const unsigned*)(xg + 512 + lane * 2);
      l_f[lane] = 0u;
      SB();
      asm volatile("s_waitcnt lgkmcnt(0)\n\ts_barrier" ::: "memory");
      SB();
      float xv[10];
#pragma unroll
      for (int i = 0; i < 4; ++i) { xv[2 * i] = bf_lo(xa[i]); xv[2 * i + 1] = bf_hi(xa[i]); }
      xv[8] = bf_lo(xb); xv[9] = bf_hi(xb);
      float sm = 0.f;
#pragma unroll
      for (int i = 0; i < 10; ++i) sm += xv[i];
      const float mean = wave_sum(sm) * (1.0f / DM);
      float sq = 0.f;
#pragma unroll
      for (int i = 0; i < 10; ++i) { const float a = xv[i] - mean; sq = fmaf(a, a, sq); }
      const float var = wave_sum(sq) * (1.0f / DM);
      const float mb = rbf(mean), rs = 1.0f / rbf(sqrtf(rbf(rbf(var) + 1e-5f)));
#pragma unroll
      for (int i = 0; i < 10; ++i) xv[i] = rbf(rbf(xv[i] - mb) * rs);
#pragma unroll
      for (int i = 0; i < 4; ++i) *(LAS f2*)(l_xn + lane * 8 + 2 * i) = f2{xv[2 * i], xv[2 * i + 1]};
      *(LAS f2*)(l_xn + 512 + lane * 2) = f2{xv[8], xv[9]};
      const u64 e1 = next_epoch(e0);
      if (lane == 0) { l_tag[0] = (unsigned)e1; l_tag[1] = (unsigned)(e1 >> 32); }
      LDS_FENCE();
      l_f[F_X] = 1u;
      if (GORG && lane == 0) put_bits(ws + WS_TS + h * 16 + s, (unsigned)(e1 >> 8), (unsigned)e1 & 0xffu, tstart << 8);
    } else {
      const bool isq = w < 3;
      if (ABL != 1 && (isq || b < 240)) {
        const int nr = isq ? 10 : 8, row0 = isq ? 80 * s : 64 * cc, stride = isq ? 12288 : 3072;
        const unsigned char* wb = isq ? wq + h * 384 + w * 128 : (w == 4 ? w1 : w3) + rgp * 128;
        const __amdgpu_buffer_rsrc_t rw = __builtin_amdgcn_make_buffer_rsrc((void*)(wb + (size_t)row0 * stride), 0, (8 * nr - 1) * stride + 128, 0x00020000);
        const int woff = rg * nr * stride + c8 * 16, woff2 = isq ? woff : OOB;
        v4u wr[10];
#pragma unroll
        for (int i = 0; i < 10; ++i) { wr[i] = ld_nt(rw, i < 8 ? woff : woff2, i * stride); if (i == 1) { SB(); asm volatile("s_barrier" ::: "memory"); SB(); } }
        SB();
        wait_flags(l_f + F_X, 1, lane, 1);
        f2 xn[5];
#pragma unroll
        for (int i = 0; i < 5; ++i) xn[i] = *(const LAS f2*)(l_xn + row0 + nr * rg + 2 * i);
        if (!isq) xn[4] = f2{0.f, 0.f};
        const unsigned t0 = __builtin_amdgcn_readfirstlane(l_tag[0]), t1 = __builtin_amdgcn_readfirstlane(l_tag[1]);
        float acc[8];
#pragma unroll
        for (int j = 0; j < 8; ++j) acc[j] = 0.f;
#pragma unroll
        for (int i = 0; i < 10; ++i)
#pragma unroll
          for (int k = 0; k < 4; ++k) { acc[2 * k] = fmaf(xn[i >> 1][i & 1], bf_lo(wr[i][k]), acc[2 * k]); acc[2 * k + 1] = fmaf(xn[i >> 1][i & 1], bf_hi(wr[i][k]), acc[2 * k + 1]); }
        const float tot = treduce8(acc, bit3);
        gu64* dst = ws + (isq ? WS_QP + (size_t)((h * 3 + w) * 8 + s) * 64 : WS_GP + (size_t)tile * 128 + (w - 4) * 64);
        put_f(dst + 8 * c8 + rg, (t0 >> 8) | (t1 << 24), t0 & 0xffu, tot);
        SB();
      } else {
        asm volatile("s_barrier" ::: "memory");
      }
    }
    STAMP(1);
  } else {
    asm volatile("s_barrier" ::: "memory");
  }
  v4u kr[12], vr[12];
  const bool paced = TLAST && w >= PW0;
  if (GORG && paced) { wait_flags(l_f + F_Q, 3, lane, 1); tstart = __builtin_amdgcn_readfirstlane(l_tag[2]); }
  const int due0 = paced ? TLAST - (((127 - ((w >> 2) * 32 + (w & 3))) * TAU16) >> 4) : -100000;
#define PACE(j) if (TLAST) { const int due = due0 + (((j) * 4 * TAU16) >> 4); \
    for (;;) { const int left = due - (int)((unsigned)__builtin_amdgcn_s_memrealtime() - tstart); if (left <= 0) break; if (left > 30) __builtin_amdgcn_s_sleep(8); else __builtin_amdgcn_s_sleep(1); } SB(); }
#define LOADV(g) { vr[3 * (g)] = ld_nt(rv, offA, (g) * 3072); vr[3 * (g) + 1] = ld_nt(rv, offA, (g) * 3072 + 1536); vr[3 * (g) + 2] = ld_nt(rv, offC, (g) * 3072); SB(); }
#pragma unroll
  for (int g = 0; g < 4; ++g) {
    PACE(g)
    kr[3 * g] = ld_nt(rk, offA, g * 3072);
    kr[3 * g + 1] = ld_nt(rk, offA, g * 3072 + 1536);
    kr[3 * g + 2] = ld_nt(rk, offC, g * 3072);
    SB();
  }
  PACE(4) LOADV(0)
  PACE(5) LOADV(1)
  if (!TLAST) { LOADV(2) LOADV(3) }
  STAMP(2);
  wait_flags(l_f + F_X, 1, lane, 1);
  const unsigned e1lo = __builtin_amdgcn_readfirstlane(l_tag[0]), e1hi = __builtin_amdgcn_readfirstlane(l_tag[1]);
  const unsigned thi = (e1lo >> 8) | (e1hi << 24), tlo = e1lo & 0xffu;
  if (w < 3) {
    const gu64* qp = ws + WS_QP + (size_t)(h * 3 + w) * 512 + lane;
    const gu64* tp = GORG && w == 0 ? ws + WS_TS + h * 16 + c8 : qp;
    u64 g[8], gt;
    for (unsigned spin = 0; spin < SPIN_CAP; ++spin) {
#pragma unroll
      for (int p = 0; p < 8; ++p) g[p] = get_g(qp + p * 64);
      gt = GORG ? get_g(tp) : g[0];
      bool ok = g_ok(gt, thi, tlo);
#pragma unroll
      for (int p = 0; p < 8; ++p) ok = ok & g_ok(g[p], thi, tlo);
      if (__all(ok)) break;
      __builtin_amdgcn_s_sleep(8);
    }
    float t = 0.f;
#pragma unroll
    for (int p = 0; p < 8; ++p) t += g_val(g[p]);
    ((LAS __bf16*)l_q)[w * 64 + lane] = (__bf16)t;
    if (GORG && w == 0) {
      int d = (int)((tstart - (unsigned)gt) << 8) >> 8;
      d = max(d, __builtin_amdgcn_mov_dpp(d, XOR1, 0xf, 0xf, true)); d = max(d, __builtin_amdgcn_mov_dpp(d, XOR2, 0xf, 0xf, true)); d = max(d, __builtin_amdgcn_mov_dpp(d, XOR7, 0xf, 0xf, true));
      l_tag[2] = tstart - (unsigned)min(max(d, 0), 300);
    }
    LDS_FENCE();
    l_f[F_Q + w] = 1u;
  }
  wait_flags(l_f + F_Q, 3, lane, 1);
  if (GORG) tstart = __builtin_amdgcn_readfirstlane(l_tag[2]);
  STAMP(3);
  {
    const v4u qa = *(const LAS v4u*)(l_q + (lane & 15) * 4);
    const v4u qc = *(const LAS v4u*)(l_q + 64 + c8 * 4);
    float xs[4];
#pragma unroll
    for (int g = 0; g < 4; ++g) {
      float sa = 0.f, sb = 0.f, sc = 0.f;
#pragma unroll
      for (int k = 0; k < 4; ++k) { sa = dot2(kr[3 * g][k], qa[k], sa); sb = dot2(kr[3 * g + 1][k], qa[k], sb); sc = dot2(kr[3 * g + 2][k], qc[k], sc); }
      const float keep = bit3 ? sb : sa, send = bit3 ? sa : sb;
      xs[g] = keep + sc + dpp<ROR8>(send);
      PIN(xs[g]);
      if (TLAST && g == 2) { SB(); PACE(6) LOADV(2) }
    }
    float x2[2];
#pragma unroll
    for (int j = 0; j < 2; ++j) { const float keep = (lane & 4) ? xs[j + 2] : xs[j], send = (lane & 4) ? xs[j] : xs[j + 2]; x2[j] = keep + dpp<XOR7>(send); }
    float sco = ((lane & 2) ? x2[1] : x2[0]) + dpp<XOR2>((lane & 2) ? x2[0] : x2[1]);
    sco += dpp<XOR1>(sco);
    sco = rbf(rbf(sco) * 0.07216878364870322f) * LOG2E;
    const float mw = wave_max(sco);
    float p = __builtin_amdgcn_exp2f(sco - mw);
    const float lw = wave_sum(p) * 0.5f;
    if (lane == 0) *(LAS f2*)(l_ml + 2 * w) = f2{mw, lw};
    LDS_ORDER();
    l_f[F_ST + w] = 1u;
    PIN(p);
    SB();
    if (TLAST) { PACE(7) LOADV(3) }
    STAMP(9);
    f2 oa[4], oc[4];
#pragma unroll
    for (int j = 0; j < 4; ++j) { oa[j] = f2{0.f, 0.f}; oc[j] = f2{0.f, 0.f}; }
#define AXPY(acc, pp, v) _Pragma("unroll") for (int k = 0; k < 4; ++k) acc[k] = __builtin_elementwise_fma(f2{pp, pp}, f2{bf_lo(v[k]), bf_hi(v[k])}, acc[k]);
#define PV_G(G) { const float pa = dpp<BCAST + 2 * (G)>(p), pb = dpp<BCAST + 8 + 2 * (G)>(p); const float pc = bit3 ? pb : pa; \
    AXPY(oa, pa, vr[3 * (G)]) AXPY(oa, pb, vr[3 * (G) + 1]) AXPY(oc, pc, vr[3 * (G) + 2]) SB(); }
    PV_G(0) PV_G(1) PV_G(2)
    float* lo = l_o + w * 192;
    LAS f2* loa = (LAS f2*)(lo + 8 * (lane & 15) + 2 * (lane >> 4));
    float* loc = lo + 128 + 8 * c8 + rg;
    const float pa = dpp<BCAST + 6>(p), pb = dpp<BCAST + 14>(p); float pc = bit3 ? pb : pa;
    PIN(pc);
    AXPY(oa, pa, vr[9]) SB();
    AXPY(oa, pb, vr[10])
    *loa = f2{swap16_add(swap32_add(oa[0].x, oa[2].x), swap32_add(oa[1].x, oa[3].x)), swap16_add(swap32_add(oa[0].y, oa[2].y), swap32_add(oa[1].y, oa[3].y))};
    SB();
    AXPY(oc, pc, vr[11])
    { const float t[8] = {oc[0].x, oc[0].y, oc[1].x, oc[1].y, oc[2].x, oc[2].y, oc[3].x, oc[3].y}; *loc = treduce8(t, bit3); }
#undef PV_G
    LDS_ORDER();
    l_f[F_DONE + w] = 1u;
  }
  STAMP(4);
  if (w >= 8 || ABL) { STAMP_FLUSH(); return; }
  if (w < 3) {
    wait_flags(l_f + F_ST, 16, lane, 1);
    const f2 ml = *(const LAS f2*)(l_ml + 2 * (lane & 15));
    float M = ml.x;
    M = fmaxf(M, dpp<XOR1>(M)); M = fmaxf(M, dpp<XOR2>(M)); M = fmaxf(M, dpp<XOR7>(M)); M = fmaxf(M, dpp<ROR8>(M));
    const float sc = __builtin_amdgcn_exp2f(ml.x - M);
    if (w == 0) {
      float L = sc * ml.y;
      L += dpp<XOR1>(L); L += dpp<XOR2>(L); L += dpp<XOR7>(L); L += dpp<ROR8>(L);
      const unsigned bits = lane == 0 ? __float_as_uint(M) : lane == 1 ? (__float_as_uint(M) << 24) : __float_as_uint(L);
      if (lane < 3) put_bits(ws + WS_ST + (size_t)b * 4 + lane, thi, tlo, bits);
    }
    const int d = w * 64 + lane;
    gu64* dst = ws + WS_OP + (size_t)((h * 3 + w) * 8 + s) * 64 + lane;
    float o = 0.f;
#define CMB(i) o = fmaf(dpp<BCAST + (i)>(sc), l_o[(i) * 192 + d], o);
    wait_flags(l_f + F_DONE, 12, lane, 0);
    CMB(0) CMB(1) CMB(2) CMB(3) CMB(4) CMB(5) CMB(6) CMB(7) CMB(8) CMB(9) CMB(10) CMB(11)
    float s12 = dpp<BCAST + 12>(sc), s13 = dpp<BCAST + 13>(sc), s14 = dpp<BCAST + 14>(sc), s15 = dpp<BCAST + 15>(sc);
    PIN(o); PIN(s12); PIN(s13); PIN(s14); PIN(s15);
    wait_flags(l_f + F_DONE + 12, 4, lane, 0);
    STAMP(5);
    o = fmaf(s12, l_o[12 * 192 + d], o); o = fmaf(s13, l_o[13 * 192 + d], o); o = fmaf(s14, l_o[14 * 192 + d], o); o = fmaf(s15, l_o[15 * 192 + d], o);
#undef CMB
    put_f(dst, thi, tlo, o);
    l_f[F_PUB + w] = 1u;
    STAMP(6);
    STAMP_FLUSH();
    return;
  }
  const int j5 = w >= 4 ? w - 4 : 4;
  if (b < 240) {
    float y;
    if (w == 3) {
      const gu64* gp = ws + WS_GP + (size_t)rgp * 1280 + lane;
      u64 g[20];
      for (unsigned spin = 0; spin < SPIN_CAP; ++spin) {
        bool ok = true;
#pragma unroll
        for (int k = 0; k < 20; ++k) g[k] = get_g(gp + k * 64);
#pragma unroll
        for (int k = 0; k < 20; ++k) ok = ok & g_ok(g[k], thi, tlo);
        if (__all(ok)) break;
        __builtin_amdgcn_s_sleep(8);
      }
      float gt = 0.f, up = 0.f;
#pragma unroll
      for (int k = 0; k < 10; ++k) { gt += g_val(g[2 * k]); up += g_val(g[2 * k + 1]); }
      gt = rbf(gt); up = rbf(up);
      y = rbf(rbf(gt / (1.0f + __expf(-gt))) * up);
    }
    const unsigned char* wb = (w >= 4 ? wo + (size_t)(256 * rgp + 64 * j5) * 1280 : w2 + (size_t)(64 * rgp) * 1280) + cc * 128;
    const __amdgpu_buffer_rsrc_t rw = __builtin_amdgcn_make_buffer_rsrc((void*)wb, 0, 63 * 1280 + 128, 0x00020000);
    if (TLAST) { for (;;) { const int left = TLAST + WOFF - (int)((unsigned)__builtin_amdgcn_s_memrealtime() - tstart); if (left <= 0) break; __builtin_amdgcn_s_sleep(4); } SB(); }
    __builtin_amdgcn_s_setprio(0);
    v4u wr[8];
#pragma unroll
    for (int i = 0; i < 8; ++i) wr[i] = ld_nt(rw, rg * 8 * 1280 + c8 * 16, i * 1280);
    SB();
    __builtin_amdgcn_s_setprio(1);
    STAMP(5);
    f2 wf[32];
    if (w >= 4) {
      const int beta = 4 * rgp + j5, hd = beta / 3;
      const gu64* op = ws + WS_OP + (size_t)beta * 512 + lane;
      const gu64* sp = ws + WS_ST + (size_t)hd * 32 + (lane & 28) + min(lane & 3, 2);
      wait_flags(l_f + F_PUB, 3, lane, 1);
      u64 g[8], gs;
      gs = get_g(sp);
#pragma unroll
      for (int p = 0; p < 8; ++p) g[p] = get_g(op + p * 64);
      SB();
#pragma unroll
      for (int i = 0; i < 8; ++i)
#pragma unroll
        for (int k = 0; k < 4; ++k) { wf[4 * i + k] = f2{bf_lo(wr[i][k]), bf_hi(wr[i][k])}; PIN(wf[4 * i + k]); }
      SB();
      for (unsigned spin = 0; spin < SPIN_CAP && !__all(g_ok(gs, thi, tlo)); ++spin) gs = get_g(sp);
      float cfs[8];
      {
        const unsigned lo = (unsigned)gs;
        const unsigned mh = __builtin_amdgcn_mov_dpp(lo, 0x00, 0xf, 0xf, true), ml = __builtin_amdgcn_mov_dpp(lo, 0x55, 0xf, 0xf, true), ll = __builtin_amdgcn_mov_dpp(lo, 0xAA, 0xf, 0xf, true);
        const float Mp = __uint_as_float((mh << 8) | ((ml >> 16) & 0xffu)), Lp = __uint_as_float(ll << 8);
        const float M = wave_max(Mp);
        float cf = __builtin_amdgcn_exp2f(Mp - M);
        float L = cf * Lp;
        L += dpp<XOR7>(L); L += dpp<ROR8>(L); L = swap16_add(L, L);
        cf = cf / L;
#pragma unroll
        for (int p = 0; p < 8; ++p) cfs[p] = __builtin_bit_cast(float, __builtin_amdgcn_readlane(__builtin_bit_cast(int, cf), 4 * p));
      }
      for (unsigned spin = 0; spin < SPIN_CAP; ++spin) {
        bool ok[8], all = true;
#pragma unroll
        for (int p = 0; p < 8; ++p) { ok[p] = g_ok(g[p], thi, tlo); all = all & ok[p]; }
        if (__all(all)) break;
#pragma unroll
        for (int p = 0; p < 8; ++p) if (!__all(ok[p])) g[p] = get_g(op + p * 64);
      }
      STAMP(6);
      y = ((cfs[0] * g_val(g[0]) + cfs[1] * g_val(g[1])) + (cfs[2] * g_val(g[2]) + cfs[3] * g_val(g[3]))) +
          ((cfs[4] * g_val(g[4]) + cfs[5] * g_val(g[5])) + (cfs[6] * g_val(g[6]) + cfs[7] * g_val(g[7])));
      y = rbf(y);
    } else {
#pragma unroll
      for (int i = 0; i < 8; ++i)
#pragma unroll
        for (int k = 0; k < 4; ++k) wf[4 * i + k] = f2{bf_lo(wr[i][k]), bf_hi(wr[i][k])};
    }
    f2 acc[4];
#pragma unroll
    for (int j = 0; j < 4; ++j) acc[j] = f2{0.f, 0.f};
#define GV(i) { const float y0 = dpp<BCAST + (i)>(y), y1 = dpp<BCAST + 8 + (i)>(y); const float yi = bit3 ? y1 : y0; \
    _Pragma("unroll") for (int k = 0; k < 4; ++k) acc[k] = __builtin_elementwise_fma(f2{yi, yi}, wf[4 * (i) + k], acc[k]); }
    GV(0) GV(1) GV(2) GV(3) GV(4) GV(5) GV(6) GV(7)
#undef GV
    { const float t[8] = {acc[0].x, acc[0].y, acc[1].x, acc[1].y, acc[2].x, acc[2].y, acc[3].x, acc[3].y}; l_gv[j5 * 64 + 8 * c8 + rg] = treduce8(t, bit3); }
    LDS_ORDER();
    unsigned old = 0;
    if (lane == 0) old = __hip_atomic_fetch_add((LAS unsigned*)(l_f + F_CNT), 1u, __ATOMIC_RELAXED, __HIP_MEMORY_SCOPE_WORKGROUP);
    asm volatile("" ::: "memory");
    const float t = ((l_gv[lane] + l_gv[64 + lane]) + (l_gv[128 + lane] + l_gv[192 + lane])) + l_gv[256 + lane];
    if (__builtin_amdgcn_readfirstlane(old) == 4u)
      put_f(ws + WS_FP + ((size_t)(cc * 8 + (lane >> 3)) * 24 + rgp) * FP_PAD + (lane & 7), thi, tlo, t);
    STAMP(7);
  } else {
    const int r = (b - 240) * 5 + j5;
    const float xres = __uint_as_float((unsigned)xg[r * 8 + c8] << 16);
    gu64* fp = ws + WS_FP + ((size_t)r * 24 + rg) * FP_PAD + c8;
    wait_flags(l_f + F_PUB, 3, lane, 1);
    STAMP(5);
    u64 g[3] = {0, 0, 0};
    for (unsigned spin = 0; spin < SPIN_CAP; ++spin) {
      bool ok = true;
#pragma unroll
      for (int k = 0; k < 3; ++k) if (spin == 0 || !g_ok(g[k], thi, tlo)) g[k] = get_rmw(fp + k * 8 * FP_PAD);
#pragma unroll
      for (int k = 0; k < 3; ++k) ok = ok & g_ok(g[k], thi, tlo);
      if (__all(ok)) break;
    }
    float t = (g_val(g[0]) + g_val(g[1])) + g_val(g[2]);
    t += dpp<ROR8>(t);
    t = swap16_add(t, t);
    t = swap32_add(t, t);
    t += xres;
    if (lane < 8) out[r * 8 + lane] = __builtin_bit_cast(u16, (__bf16)t);
    if (r == 0 && lane == 0) __hip_atomic_store(ws + WS_EPOCH, ((u64)e1hi << 32) | e1lo, RLX_AGENT);
    STAMP(8);
  }
  STAMP_FLUSH();
}

extern "C" void kernel_launch(void* const* d_in, const int* in_sizes, int n_in, void* d_out, int out_size, void* d_ws, size_t ws_size,
                              hipStream_t stream) {
  (void)in_sizes; (void)n_in; (void)out_size; (void)ws_size;
  fused_decode<<<dim3(NWG), dim3(NTHR), 0, stream>>>((const u16*)d_in[0], (const unsigned char*)d_in[1], (const unsigned char*)d_in[2],
                                                      (const unsigned char*)d_in[3], (const unsigned char*)d_in[4], (const unsigned char*)d_in[6],
                                                      (const unsigned char*)d_in[7], (const unsigned char*)d_in[8], (u16*)d_out, (u64*)d_ws);
}
```

```cpp
#include <hip/hip_runtime.h>
#include <hip/hip_bf16.h>

typedef unsigned v4u __attribute__((ext_vector_type(4)));
typedef float f2 __attribute__((ext_vector_type(2)));
typedef unsigned long long u64;
typedef unsigned short u16;
typedef __attribute__((ext_vector_type(2))) __bf16 bf16x2;
#define GAS __attribute__((address_space(1)))
#define LAS __attribute__((address_space(3)))
typedef GAS u64 gu64;
#define RLX_AGENT __ATOMIC_RELAXED, __HIP_MEMORY_SCOPE_AGENT

#define SLEEP_LONG 4
#define SLEEP_SHORT 0

constexpr int DM = 640, SK = 32768, WIN = 4096;
constexpr int NWG = 256, NTHR = 1024;
constexpr int ROWB = 384;
constexpr int FP_PAD = 16;
constexpr size_t WS_EPOCH = 0;
constexpr size_t WS_QP = 64;
constexpr size_t WS_GP = WS_QP + (size_t)NWG * 192;
constexpr size_t WS_OP = WS_GP + (size_t)240 * 128;
constexpr size_t WS_ST = WS_OP + (size_t)NWG * 192;
constexpr size_t WS_FP = WS_ST + (size_t)NWG * 4;
constexpr unsigned SPIN_CAP = 1u << 18;
constexpr float LOG2E = 1.4426950408889634f;
constexpr int OOB = 0x7fffff00;

__device__ __forceinline__ float bf_lo(unsigned d) { return __uint_as_float(d << 16); }
__device__ __forceinline__ float bf_hi(unsigned d) { return __uint_as_float(d & 0xffff0000u); }
__device__ __forceinline__ float rbf(float x) { return (float)(__bf16)x; }
__device__ __forceinline__ float dot2(unsigned a, unsigned b, float acc) {
  return __builtin_amdgcn_fdot2_f32_bf16(__builtin_bit_cast(bf16x2, a), __builtin_bit_cast(bf16x2, b), acc, false);
}
template <int CTRL> __device__ __forceinline__ float dpp(float x) {
  return __builtin_bit_cast(float, __builtin_amdgcn_mov_dpp(__builtin_bit_cast(int, x), CTRL, 0xf, 0xf, true));
}
constexpr int XOR1 = 0xB1, XOR2 = 0x4E, XOR7 = 0x141, ROR8 = 0x128, BCAST = 0x150;
__device__ __forceinline__ float swap32_add(float a, float b) {
  auto r = __builtin_amdgcn_permlane32_swap(__float_as_uint(a), __float_as_uint(b), false, false);
  return __uint_as_float(r[0]) + __uint_as_float(r[1]);
}
__device__ __forceinline__ float swap16_add(float a, float b) {
  auto r = __builtin_amdgcn_permlane16_swap(__float_as_uint(a), __float_as_uint(b), false, false);
  return __uint_as_float(r[0]) + __uint_as_float(r[1]);
}
__device__ __forceinline__ float wave_sum(float x) {
  x += dpp<XOR1>(x); x += dpp<XOR2>(x); x += dpp<XOR7>(x); x += dpp<ROR8>(x);
  x = swap16_add(x, x); x = swap32_add(x, x);
  return x;
}
__device__ __forceinline__ float wave_max(float x) {
  x = fmaxf(x, dpp<XOR1>(x)); x = fmaxf(x, dpp<XOR2>(x)); x = fmaxf(x, dpp<XOR7>(x)); x = fmaxf(x, dpp<ROR8>(x));
  auto r = __builtin_amdgcn_permlane16_swap(__float_as_uint(x), __float_as_uint(x), false, false);
  x = fmaxf(__uint_as_float(r[0]), __uint_as_float(r[1]));
  auto t = __builtin_amdgcn_permlane32_swap(__float_as_uint(x), __float_as_uint(x), false, false);
  return fmaxf(__uint_as_float(t[0]), __uint_as_float(t[1]));
}
__device__ __forceinline__ float treduce8(const f2 (&a)[4], int bit3) {
  const float e = swap16_add(swap32_add(a[0].x, a[2].x), swap32_add(a[1].x, a[3].x));
  const float o = swap16_add(swap32_add(a[0].y, a[2].y), swap32_add(a[1].y, a[3].y));
  return (bit3 ? o : e) + dpp<ROR8>(bit3 ? e : o);
}
__device__ __forceinline__ void axpy(f2 (&acc)[4], float p, v4u v) {
#pragma unroll
  for (int k = 0; k < 4; ++k) acc[k] = __builtin_elementwise_fma(f2{p, p}, f2{bf_lo(v[k]), bf_hi(v[k])}, acc[k]);
}
__device__ __forceinline__ void put_bits(gu64* g, unsigned thi, unsigned tlo, unsigned bits) {
  const unsigned lo = __builtin_amdgcn_alignbit(tlo, bits, 8);
  __hip_atomic_store(g, ((u64)thi << 32) | lo, RLX_AGENT);
}
__device__ __forceinline__ void put_f(gu64* g, unsigned thi, unsigned tlo, float v) { put_bits(g, thi, tlo, __float_as_uint(v)); }
__device__ __forceinline__ u64 get_g(const gu64* g) { return __hip_atomic_load(const_cast<gu64*>(g), RLX_AGENT); }
__device__ __forceinline__ u64 get_rmw(gu64* g) { u64 z = 0; asm volatile("" : "+v"(z)); return __hip_atomic_fetch_or(g, z, RLX_AGENT); }
__device__ __forceinline__ bool g_ok(u64 x, unsigned thi, unsigned tlo) { return ((unsigned)(x >> 32) == thi) & (((unsigned)x >> 24) == tlo); }
__device__ __forceinline__ float g_val(u64 x) { return __uint_as_float((unsigned)x << 8); }
__device__ __forceinline__ u64 next_epoch(u64 e0) {
  const u64 e = ((u64)(unsigned)__builtin_amdgcn_readfirstlane((unsigned)(e0 >> 32)) << 32) | (unsigned)__builtin_amdgcn_readfirstlane((unsigned)e0);
  return e * 6364136223846793005ull + 1442695040888963407ull;
}
__device__ __forceinline__ v4u ld_nt(__amdgpu_buffer_rsrc_t r, int voff, int soff) { return __builtin_amdgcn_raw_buffer_load_b128(r, voff, soff, 2); }
#define SB() __builtin_amdgcn_sched_barrier(0)
#define PIN(x) asm volatile("" : "+v"(x))
#define LDS_ORDER() asm volatile("" ::: "memory")
#define LDS_FENCE() asm volatile("s_waitcnt lgkmcnt(0)" ::: "memory")

constexpr int F_DONE = 0, F_ST = 16, F_Q = 32, F_PUB = 36, F_X = 40, F_CNT = 41;

__device__ __forceinline__ void wait_flags(volatile LAS unsigned* f, int n, int lane, int sleep) {
  const int i = lane < n ? lane : 0;
  while (!__all(f[i] != 0u)) { if (sleep) __builtin_amdgcn_s_sleep(SLEEP_LONG); else __builtin_amdgcn_s_sleep(SLEEP_SHORT); }
  asm volatile("" ::: "memory");
}

__global__ void __launch_bounds__(NTHR, 4)
fused_decode(const u16* __restrict__ xg, const unsigned char* __restrict__ Kc, const unsigned char* __restrict__ Vc,
             const unsigned char* __restrict__ wq, const unsigned char* __restrict__ wo, const unsigned char* __restrict__ w1,
             const unsigned char* __restrict__ w3, const unsigned char* __restrict__ w2, u16* __restrict__ out, u64* __restrict__ wsp) {
  __shared__ __attribute__((aligned(16))) float lds[16 * 192 + 96 + 32 + 320 + 648 + 64 + 4];
  float* l_o = lds;
  unsigned* l_q = (unsigned*)(lds + 3072);
  float* l_ml = lds + 3072 + 96;
  float* l_gv = lds + 3072 + 128;
  float* l_xn = lds + 3072 + 448;
  volatile LAS unsigned* l_f = (volatile LAS unsigned*)(lds + 3072 + 1096);
  volatile LAS unsigned* l_tag = (volatile LAS unsigned*)(lds + 3072 + 1160);

  const int b = blockIdx.x, tid = threadIdx.x, lane = tid & 63, w = __builtin_amdgcn_readfirstlane(tid >> 6);
  const int h = b >> 3, s = b & 7;
  const int tile = s * 30 + h;
  const int rgp = tile / 10, cc = tile - rgp * 10;
  gu64* ws = (gu64*)wsp;
  const size_t kvbase = ((size_t)h * SK + (SK - WIN) + s * 512 + w * 32) * ROWB;
  const __amdgpu_buffer_rsrc_t rk = __builtin_amdgcn_make_buffer_rsrc((void*)(Kc + kvbase), 0, 32 * ROWB, 0x00020000);
  const __amdgpu_buffer_rsrc_t rv = __builtin_amdgcn_make_buffer_rsrc((void*)(Vc + kvbase), 0, 32 * ROWB, 0x00020000);
  const int bit3 = (lane >> 3) & 1, rg = lane >> 3, c8 = lane & 7;
  const int offA = (lane >> 4) * ROWB + (lane & 15) * 16;
  const int offC = ((lane >> 4) + 4 * bit3) * ROWB + 256 + c8 * 16;

  __builtin_amdgcn_s_setprio(1);
  if (w < 6) {
    if (w == 3) {
      int vz = 0; PIN(vz);
      const u64 e0 = get_g(ws + WS_EPOCH + vz);
      const v4u xa = *(const v4u*)(xg + lane * 8);
      const unsigned xb = *(const unsigned*)(xg + 512 + lane * 2);
      l_f[lane] = 0u;
      SB();
      asm volatile("s_waitcnt lgkmcnt(0)\n\ts_barrier" ::: "memory");
      SB();
      float xv[10];
#pragma unroll
      for (int i = 0; i < 4; ++i) { xv[2 * i] = bf_lo(xa[i]); xv[2 * i + 1] = bf_hi(xa[i]); }
      xv[8] = bf_lo(xb); xv[9] = bf_hi(xb);
      float sm = 0.f;
#pragma unroll
      for (int i = 0; i < 10; ++i) sm += xv[i];
      const float mean = wave_sum(sm) * (1.0f / DM);
      float sq = 0.f;
#pragma unroll
      for (int i = 0; i < 10; ++i) { const float a = xv[i] - mean; sq = fmaf(a, a, sq); }
      const float var = wave_sum(sq) * (1.0f / DM);
      const float mb = rbf(mean), rs = 1.0f / rbf(sqrtf(rbf(rbf(var) + 1e-5f)));
#pragma unroll
      for (int i = 0; i < 10; ++i) xv[i] = rbf(rbf(xv[i] - mb) * rs);
#pragma unroll
      for (int i = 0; i < 4; ++i) *(LAS f2*)(l_xn + lane * 8 + 2 * i) = f2{xv[2 * i], xv[2 * i + 1]};
      *(LAS f2*)(l_xn + 512 + lane * 2) = f2{xv[8], xv[9]};
      const u64 e1 = next_epoch(e0);
      if (lane == 0) { l_tag[0] = (unsigned)e1; l_tag[1] = (unsigned)(e1 >> 32); }
      LDS_FENCE();
      l_f[F_X] = 1u;
    } else {
      const bool isq = w < 3;
      if (isq || b < 240) {
        const int nr = isq ? 10 : 8, row0 = isq ? 80 * s : 64 * cc, stride = isq ? 12288 : 3072;
        const unsigned char* wb = isq ? wq + h * 384 + w * 128 : (w == 4 ? w1 : w3) + rgp * 128;
        const __amdgpu_buffer_rsrc_t rw = __builtin_amdgcn_make_buffer_rsrc((void*)(wb + (size_t)row0 * stride), 0, (8 * nr - 1) * stride + 128, 0x00020000);
        const int woff = rg * nr * stride + c8 * 16, woff2 = isq ? woff : OOB;
        v4u wr[10];
#pragma unroll
        for (int i = 0; i < 10; ++i) { wr[i] = ld_nt(rw, i < 8 ? woff : woff2, i * stride); if (i == 1) { SB(); asm volatile("s_barrier" ::: "memory"); SB(); } }
        SB();
        wait_flags(l_f + F_X, 1, lane, 1);
        f2 xn[5];
#pragma unroll
        for (int i = 0; i < 5; ++i) xn[i] = *(const LAS f2*)(l_xn + row0 + nr * rg + 2 * i);
        if (!isq) xn[4] = f2{0.f, 0.f};
        const unsigned t0 = __builtin_amdgcn_readfirstlane(l_tag[0]), t1 = __builtin_amdgcn_readfirstlane(l_tag[1]);
        f2 acc[4];
#pragma unroll
        for (int j = 0; j < 4; ++j) acc[j] = f2{0.f, 0.f};
#pragma unroll
        for (int i = 0; i < 10; ++i) axpy(acc, xn[i >> 1][i & 1], wr[i]);
        gu64* dst = ws + (isq ? WS_QP + (size_t)((h * 3 + w) * 8 + s) * 64 : WS_GP + (size_t)tile * 128 + (w - 4) * 64);
        put_f(dst + 8 * c8 + rg, (t0 >> 8) | (t1 << 24), t0 & 0xffu, treduce8(acc, bit3));
        SB();
      } else {
        asm volatile("s_barrier" ::: "memory");
      }
    }
  } else {
    asm volatile("s_barrier" ::: "memory");
  }
  v4u kr[12], vr[12];
#pragma unroll
  for (int g = 0; g < 4; ++g) {
    kr[3 * g] = ld_nt(rk, offA, g * 3072);
    kr[3 * g + 1] = ld_nt(rk, offA, g * 3072 + 1536);
    kr[3 * g + 2] = ld_nt(rk, offC, g * 3072);
    SB();
  }
#pragma unroll
  for (int g = 0; g < 4; ++g) {
    vr[3 * g] = ld_nt(rv, offA, g * 3072);
    vr[3 * g + 1] = ld_nt(rv, offA, g * 3072 + 1536);
    vr[3 * g + 2] = ld_nt(rv, offC, g * 3072);
    SB();
  }
  wait_flags(l_f + F_X, 1, lane, 1);
  const unsigned e1lo = __builtin_amdgcn_readfirstlane(l_tag[0]), e1hi = __builtin_amdgcn_readfirstlane(l_tag[1]);
  const unsigned thi = (e1lo >> 8) | (e1hi << 24), tlo = e1lo & 0xffu;
  if (w < 3) {
    const gu64* qp = ws + WS_QP + (size_t)(h * 3 + w) * 512 + lane;
    u64 g[8];
#pragma nounroll
    for (unsigned spin = 0; spin < SPIN_CAP; ++spin) {
      bool ok = true;
#pragma unroll
      for (int p = 0; p < 8; ++p) g[p] = get_g(qp + p * 64);
#pragma unroll
      for (int p = 0; p < 8; ++p) ok = ok & g_ok(g[p], thi, tlo);
      if (__all(ok)) break;
      __builtin_amdgcn_s_sleep(8);
    }
    float t = 0.f;
#pragma unroll
    for (int p = 0; p < 8; ++p) t += g_val(g[p]);
    ((LAS __bf16*)l_q)[w * 64 + lane] = (__bf16)t;
    LDS_FENCE();
    l_f[F_Q + w] = 1u;
  }
  wait_flags(l_f + F_Q, 3, lane, 1);
  {
    const v4u qa = *(const LAS v4u*)(l_q + (lane & 15) * 4);
    const v4u qc = *(const LAS v4u*)(l_q + 64 + c8 * 4);
    float xs[4];
#pragma unroll
    for (int g = 0; g < 4; ++g) {
      float sa = 0.f, sb = 0.f, sc = 0.f;
#pragma unroll
      for (int k = 0; k < 4; ++k) { sa = dot2(kr[3 * g][k], qa[k], sa); sb = dot2(kr[3 * g + 1][k], qa[k], sb); sc = dot2(kr[3 * g + 2][k], qc[k], sc); }
      const float keep = bit3 ? sb : sa, send = bit3 ? sa : sb;
      xs[g] = keep + sc + dpp<ROR8>(send);
      PIN(xs[g]);
    }
    float x2[2];
#pragma unroll
    for (int j = 0; j < 2; ++j) { const float keep = (lane & 4) ? xs[j + 2] : xs[j], send = (lane & 4) ? xs[j] : xs[j + 2]; x2[j] = keep + dpp<XOR7>(send); }
    float sco = ((lane & 2) ? x2[1] : x2[0]) + dpp<XOR2>((lane & 2) ? x2[0] : x2[1]);
    sco += dpp<XOR1>(sco);
    sco = rbf(rbf(sco) * 0.07216878364870322f) * LOG2E;
    const float mw = wave_max(sco);
    float p = __builtin_amdgcn_exp2f(sco - mw);
    const float lw = wave_sum(p) * 0.5f;
    if (lane == 0) *(LAS f2*)(l_ml + 2 * w) = f2{mw, lw};
    LDS_ORDER();
    l_f[F_ST + w] = 1u;
    PIN(p);
    SB();
    f2 oa[4], oc[4];
#pragma unroll
    for (int j = 0; j < 4; ++j) { oa[j] = f2{0.f, 0.f}; oc[j] = f2{0.f, 0.f}; }
#define PV_G(G) { const float pa = dpp<BCAST + 2 * (G)>(p), pb = dpp<BCAST + 8 + 2 * (G)>(p); \
    axpy(oa, pa, vr[3 * (G)]); axpy(oa, pb, vr[3 * (G) + 1]); axpy(oc, bit3 ? pb : pa, vr[3 * (G) + 2]); SB(); }
    PV_G(0) PV_G(1) PV_G(2)
#undef PV_G
    float* lo = l_o + w * 192;
    LAS f2* loa = (LAS f2*)(lo + 8 * (lane & 15) + 2 * (lane >> 4));
    float* loc = lo + 128 + 8 * c8 + rg;
    const float pa = dpp<BCAST + 6>(p), pb = dpp<BCAST + 14>(p); float pc = bit3 ? pb : pa;
    PIN(pc);
    axpy(oa, pa, vr[9]); SB();
    axpy(oa, pb, vr[10]);
    *loa = f2{swap16_add(swap32_add(oa[0].x, oa[2].x), swap32_add(oa[1].x, oa[3].x)), swap16_add(swap32_add(oa[0].y, oa[2].y), swap32_add(oa[1].y, oa[3].y))};
    SB();
    axpy(oc, pc, vr[11]);
    *loc = treduce8(oc, bit3);
    LDS_ORDER();
    l_f[F_DONE + w] = 1u;
  }
  if (w >= 8) return;
  if (w < 3) {
    wait_flags(l_f + F_ST, 16, lane, 1);
    const f2 ml = *(const LAS f2*)(l_ml + 2 * (lane & 15));
    float M = ml.x;
    M = fmaxf(M, dpp<XOR1>(M)); M = fmaxf(M, dpp<XOR2>(M)); M = fmaxf(M, dpp<XOR7>(M)); M = fmaxf(M, dpp<ROR8>(M));
    const float sc = __builtin_amdgcn_exp2f(ml.x - M);
    if (w == 0) {
      float L = sc * ml.y;
      L += dpp<XOR1>(L); L += dpp<XOR2>(L); L += dpp<XOR7>(L); L += dpp<ROR8>(L);
      const unsigned bits = lane == 0 ? __float_as_uint(M) : lane == 1 ? (__float_as_uint(M) << 24) : __float_as_uint(L);
      if (lane < 3) put_bits(ws + WS_ST + (size_t)b * 4 + lane, thi, tlo, bits);
    }
    const int d = w * 64 + lane;
    gu64* dst = ws + WS_OP + (size_t)((h * 3 + w) * 8 + s) * 64 + lane;
    float o = 0.f;
#define CMB(i) o = fmaf(dpp<BCAST + (i)>(sc), l_o[(i) * 192 + d], o);
    wait_flags(l_f + F_DONE, 12, lane, 0);
    CMB(0) CMB(1) CMB(2) CMB(3) CMB(4) CMB(5) CMB(6) CMB(7) CMB(8) CMB(9) CMB(10) CMB(11)
#undef CMB
    float s12 = dpp<BCAST + 12>(sc), s13 = dpp<BCAST + 13>(sc), s14 = dpp<BCAST + 14>(sc), s15 = dpp<BCAST + 15>(sc);
    PIN(o); PIN(s12); PIN(s13); PIN(s14); PIN(s15);
    wait_flags(l_f + F_DONE + 12, 4, lane, 0);
    o = fmaf(s12, l_o[12 * 192 + d], o); o = fmaf(s13, l_o[13 * 192 + d], o); o = fmaf(s14, l_o[14 * 192 + d], o); o = fmaf(s15, l_o[15 * 192 + d], o);
    put_f(dst, thi, tlo, o);
    l_f[F_PUB + w] = 1u;
    return;
  }
  const int j5 = w >= 4 ? w - 4 : 4;
  if (b < 240) {
    float y;
    if (w == 3) {
      const gu64* gp = ws + WS_GP + (size_t)rgp * 1280 + lane;
      u64 g[20];
#pragma nounroll
      for (unsigned spin = 0; spin < SPIN_CAP; ++spin) {
        bool ok = true;
#pragma unroll
        for (int k = 0; k < 20; ++k) g[k] = get_g(gp + k * 64);
#pragma unroll
        for (int k = 0; k < 20; ++k) ok = ok & g_ok(g[k], thi, tlo);
        if (__all(ok)) break;
        __builtin_amdgcn_s_sleep(8);
      }
      float gt = 0.f, up = 0.f;
#pragma unroll
      for (int k = 0; k < 10; ++k) { gt += g_val(g[2 * k]); up += g_val(g[2 * k + 1]); }
      gt = rbf(gt); up = rbf(up);
      y = rbf(rbf(gt / (1.0f + __expf(-gt))) * up);
    }
    const unsigned char* wb = (w >= 4 ? wo + (size_t)(256 * rgp + 64 * j5) * 1280 : w2 + (size_t)(64 * rgp) * 1280) + cc * 128;
    const __amdgpu_buffer_rsrc_t rw = __builtin_amdgcn_make_buffer_rsrc((void*)wb, 0, 63 * 1280 + 128, 0x00020000);
    __builtin_amdgcn_s_setprio(0);
    v4u wr[8];
#pragma unroll
    for (int i = 0; i < 8; ++i) wr[i] = ld_nt(rw, rg * 8 * 1280 + c8 * 16, i * 1280);
    SB();
    __builtin_amdgcn_s_setprio(1);
    f2 wf[32];
    if (w >= 4) {
      const int beta = 4 * rgp + j5, hd = beta / 3;
      const gu64* op = ws + WS_OP + (size_t)beta * 512 + lane;
      const gu64* sp = ws + WS_ST + (size_t)hd * 32 + (lane & 28) + min(lane & 3, 2);
      wait_flags(l_f + F_PUB, 3, lane, 1);
      u64 g[8], gs;
      gs = get_g(sp);
#pragma unroll
      for (int p = 0; p < 8; ++p) g[p] = get_g(op + p * 64);
      SB();
#pragma unroll
      for (int i = 0; i < 8; ++i) PIN(wr[i]);
#pragma unroll
      for (int i = 0; i < 8; ++i)
#pragma unroll
        for (int k = 0; k < 4; ++k) { wf[4 * i + k] = f2{bf_lo(wr[i][k]), bf_hi(wr[i][k])}; PIN(wf[4 * i + k]); }
      SB();
#pragma nounroll
      for (unsigned spin = 0; spin < SPIN_CAP && !__all(g_ok(gs, thi, tlo)); ++spin) gs = get_g(sp);
      float cfs[8];
      {
        const unsigned lo = (unsigned)gs;
        const unsigned mh = __builtin_amdgcn_mov_dpp(lo, 0x00, 0xf, 0xf, true), ml = __builtin_amdgcn_mov_dpp(lo, 0x55, 0xf, 0xf, true), ll = __builtin_amdgcn_mov_dpp(lo, 0xAA, 0xf, 0xf, true);
        const float Mp = __uint_as_float((mh << 8) | ((ml >> 16) & 0xffu)), Lp = __uint_as_float(ll << 8);
        const float M = wave_max(Mp);
        float cf = __builtin_amdgcn_exp2f(Mp - M);
        float L = cf * Lp;
        L += dpp<XOR7>(L); L += dpp<ROR8>(L); L = swap16_add(L, L);
        cf = cf / L;
#pragma unroll
        for (int p = 0; p < 8; ++p) cfs[p] = __builtin_bit_cast(float, __builtin_amdgcn_readlane(__builtin_bit_cast(int, cf), 4 * p));
      }
#pragma nounroll
      for (unsigned spin = 0; spin < SPIN_CAP; ++spin) {
        bool ok[8], all = true;
#pragma unroll
        for (int p = 0; p < 8; ++p) { ok[p] = g_ok(g[p], thi, tlo); all = all & ok[p]; }
        if (__all(all)) break;
#pragma unroll
        for (int p = 0; p < 8; ++p) if (!__all(ok[p])) g[p] = get_g(op + p * 64);
      }
      y = ((cfs[0] * g_val(g[0]) + cfs[1] * g_val(g[1])) + (cfs[2] * g_val(g[2]) + cfs[3] * g_val(g[3]))) +
          ((cfs[4] * g_val(g[4]) + cfs[5] * g_val(g[5])) + (cfs[6] * g_val(g[6]) + cfs[7] * g_val(g[7])));
      y = rbf(y);
    } else {
#pragma unroll
      for (int i = 0; i < 8; ++i)
#pragma unroll
        for (int k = 0; k < 4; ++k) wf[4 * i + k] = f2{bf_lo(wr[i][k]), bf_hi(wr[i][k])};
    }
    f2 acc[4];
#pragma unroll
    for (int j = 0; j < 4; ++j) acc[j] = f2{0.f, 0.f};
#define GV(i) { const float y0 = dpp<BCAST + (i)>(y), y1 = dpp<BCAST + 8 + (i)>(y); const float yi = bit3 ? y1 : y0; \
    _Pragma("unroll") for (int k = 0; k < 4; ++k) acc[k] = __builtin_elementwise_fma(f2{yi, yi}, wf[4 * (i) + k], acc[k]); }
    GV(0) GV(1) GV(2) GV(3) GV(4) GV(5) GV(6) GV(7)
#undef GV
    l_gv[j5 * 64 + 8 * c8 + rg] = treduce8(acc, bit3);
    LDS_ORDER();
    unsigned old = 0;
    if (lane == 0) old = __hip_atomic_fetch_add((LAS unsigned*)(l_f + F_CNT), 1u, __ATOMIC_RELAXED, __HIP_MEMORY_SCOPE_WORKGROUP);
    LDS_ORDER();
    const float t = ((l_gv[lane] + l_gv[64 + lane]) + (l_gv[128 + lane] + l_gv[192 + lane])) + l_gv[256 + lane];
    if (__builtin_amdgcn_readfirstlane(old) == 4u)
      put_f(ws + WS_FP + ((size_t)(cc * 8 + rg) * 24 + rgp) * FP_PAD + c8, thi, tlo, t);
  } else {
    const int r = (b - 240) * 5 + j5;
    const float xres = __uint_as_float((unsigned)xg[r * 8 + c8] << 16);
    gu64* fp = ws + WS_FP + ((size_t)r * 24 + rg) * FP_PAD + c8;
    wait_flags(l_f + F_PUB, 3, lane, 1);
    u64 g[3] = {0, 0, 0};
#pragma nounroll
    for (unsigned spin = 0; spin < SPIN_CAP; ++spin) {
      bool ok = true;
#pragma unroll
      for (int k = 0; k < 3; ++k) if (spin == 0 || !g_ok(g[k], thi, tlo)) g[k] = get_rmw(fp + k * 8 * FP_PAD);
#pragma unroll
      for (int k = 0; k < 3; ++k) ok = ok & g_ok(g[k], thi, tlo);
      if (__all(ok)) break;
    }
    float t = (g_val(g[0]) + g_val(g[1])) + g_val(g[2]);
    t += dpp<ROR8>(t);
    t = swap16_add(t, t);
    t = swap32_add(t, t);
    t += xres;
    if (lane < 8) out[r * 8 + lane] = __builtin_bit_cast(u16, (__bf16)t);
    if (r == 0 && lane == 0) __hip_atomic_store(ws + WS_EPOCH, ((u64)e1hi << 32) | e1lo, RLX_AGENT);
  }
}

extern "C" void kernel_launch(void* const* d_in, const int* in_sizes, int n_in, void* d_out, int out_size, void* d_ws, size_t ws_size,
                              hipStream_t stream) {
  (void)in_sizes; (void)n_in; (void)out_size; (void)ws_size;
  fused_decode<<<dim3(NWG), dim3(NTHR), 0, stream>>>((const u16*)d_in[0], (const unsigned char*)d_in[1], (const unsigned char*)d_in[2],
                                                      (const unsigned char*)d_in[3], (const unsigned char*)d_in[4], (const unsigned char*)d_in[6],
                                                      (const unsigned char*)d_in[7], (const unsigned char*)d_in[8], (u16*)d_out, (u64*)d_ws);
}
```

```cpp
#include <hip/hip_runtime.h>
#include <hip/hip_bf16.h>

typedef unsigned v4u __attribute__((ext_vector_type(4)));
typedef float f2 __attribute__((ext_vector_type(2)));
typedef unsigned long long u64;
typedef unsigned short u16;
typedef __attribute__((ext_vector_type(2))) __bf16 bf16x2;
#define GAS __attribute__((address_space(1)))
#define LAS __attribute__((address_space(3)))
typedef GAS u64 gu64;
#define RLX_AGENT __ATOMIC_RELAXED, __HIP_MEMORY_SCOPE_AGENT

constexpr int DM = 640, SK = 32768, WIN = 4096;
constexpr int NWG = 256, NTHR = 1024;
constexpr int ROWB = 384;
constexpr int FP_PAD = 16;
constexpr size_t WS_EPOCH = 0;
constexpr size_t WS_QP = 64;
constexpr size_t WS_GP = WS_QP + (size_t)NWG * 192;
constexpr size_t WS_OP = WS_GP + (size_t)240 * 128;
constexpr size_t WS_ST = WS_OP + (size_t)NWG * 192;
constexpr size_t WS_FP = WS_ST + (size_t)NWG * 4;
constexpr unsigned SPIN_CAP = 1u << 18;
constexpr float LOG2E = 1.4426950408889634f;
constexpr int OOB = 0x7fffff00;

__device__ __forceinline__ float bf_lo(unsigned d) { return __uint_as_float(d << 16); }
__device__ __forceinline__ float bf_hi(unsigned d) { return __uint_as_float(d & 0xffff0000u); }
__device__ __forceinline__ float rbf(float x) { return (float)(__bf16)x; }
__device__ __forceinline__ float dot2(unsigned a, unsigned b, float acc) {
  return __builtin_amdgcn_fdot2_f32_bf16(__builtin_bit_cast(bf16x2, a), __builtin_bit_cast(bf16x2, b), acc, false);
}
template <int CTRL> __device__ __forceinline__ float dpp(float x) {
  return __builtin_bit_cast(float, __builtin_amdgcn_mov_dpp(__builtin_bit_cast(int, x), CTRL, 0xf, 0xf, true));
}
constexpr int XOR1 = 0xB1, XOR2 = 0x4E, XOR7 = 0x141, ROR8 = 0x128, BCAST = 0x150;
__device__ __forceinline__ float swap32_add(float a, float b) {
  auto r = __builtin_amdgcn_permlane32_swap(__float_as_uint(a), __float_as_uint(b), false, false);
  return __uint_as_float(r[0]) + __uint_as_float(r[1]);
}
__device__ __forceinline__ float swap16_add(float a, float b) {
  auto r = __builtin_amdgcn_permlane16_swap(__float_as_uint(a), __float_as_uint(b), false, false);
  return __uint_as_float(r[0]) + __uint_as_float(r[1]);
}
__device__ __forceinline__ float wave_sum(float x) {
  x += dpp<XOR1>(x); x += dpp<XOR2>(x); x += dpp<XOR7>(x); x += dpp<ROR8>(x);
  x = swap16_add(x, x); x = swap32_add(x, x);
  return x;
}
__device__ __forceinline__ float wave_max(float x) {
  x = fmaxf(x, dpp<XOR1>(x)); x = fmaxf(x, dpp<XOR2>(x)); x = fmaxf(x, dpp<XOR7>(x)); x = fmaxf(x, dpp<ROR8>(x));
  auto r = __builtin_amdgcn_permlane16_swap(__float_as_uint(x), __float_as_uint(x), false, false);
  x = fmaxf(__uint_as_float(r[0]), __uint_as_float(r[1]));
  auto t = __builtin_amdgcn_permlane32_swap(__float_as_uint(x), __float_as_uint(x), false, false);
  return fmaxf(__uint_as_float(t[0]), __uint_as_float(t[1]));
}
__device__ __forceinline__ float treduce8(const f2 (&a)[4], int bit3) {
  const float e = swap16_add(swap32_add(a[0].x, a[2].x), swap32_add(a[1].x, a[3].x));
  const float o = swap16_add(swap32_add(a[0].y, a[2].y), swap32_add(a[1].y, a[3].y));
  return (bit3 ? o : e) + dpp<ROR8>(bit3 ? e : o);
}
__device__ __forceinline__ void axpy(f2 (&acc)[4], float p, v4u v) {
#pragma unroll
  for (int k = 0; k < 4; ++k) acc[k] = __builtin_elementwise_fma(f2{p, p}, f2{bf_lo(v[k]), bf_hi(v[k])}, acc[k]);
}
__device__ __forceinline__ void put_bits(gu64* g, unsigned thi, unsigned tlo, unsigned bits) {
  const unsigned lo = __builtin_amdgcn_alignbit(tlo, bits, 8);
  __hip_atomic_store(g, ((u64)thi << 32) | lo, RLX_AGENT);
}
__device__ __forceinline__ void put_f(gu64* g, unsigned thi, unsigned tlo, float v) { put_bits(g, thi, tlo, __float_as_uint(v)); }
__device__ __forceinline__ u64 get_g(const gu64* g) { return __hip_atomic_load(const_cast<gu64*>(g), RLX_AGENT); }
__device__ __forceinline__ u64 get_rmw(gu64* g) { u64 z = 0; asm volatile("" : "+v"(z)); return __hip_atomic_fetch_or(g, z, RLX_AGENT); }
__device__ __forceinline__ bool g_ok(u64 x, unsigned thi, unsigned tlo) { return ((unsigned)(x >> 32) == thi) & (((unsigned)x >> 24) == tlo); }
__device__ __forceinline__ float g_val(u64 x) { return __uint_as_float((unsigned)x << 8); }
__device__ __forceinline__ u64 next_epoch(u64 e0) {
  const u64 e = ((u64)(unsigned)__builtin_amdgcn_readfirstlane((unsigned)(e0 >> 32)) << 32) | (unsigned)__builtin_amdgcn_readfirstlane((unsigned)e0);
  return e * 6364136223846793005ull + 1442695040888963407ull;
}
__device__ __forceinline__ v4u ld_nt(__amdgpu_buffer_rsrc_t r, int voff, int soff) { return __builtin_amdgcn_raw_buffer_load_b128(r, voff, soff, 19); }
#define SB() __builtin_amdgcn_sched_barrier(0)
#define PIN(x) asm volatile("" : "+v"(x))
#define LDS_ORDER() asm volatile("" ::: "memory")
#define LDS_FENCE() asm volatile("s_waitcnt lgkmcnt(0)" ::: "memory")

constexpr int F_DONE = 0, F_ST = 16, F_Q = 32, F_PUB = 36, F_X = 40, F_CNT = 41;

__device__ __forceinline__ void wait_flags(volatile LAS unsigned* f, int n, int lane, int sleep) {
  const int i = lane < n ? lane : 0;
  while (!__all(f[i] != 0u)) { if (sleep) __builtin_amdgcn_s_sleep(4); else __builtin_amdgcn_s_sleep(0); }
  asm volatile("" ::: "memory");
}

__global__ void __launch_bounds__(NTHR, 4)
fused_decode(const u16* __restrict__ xg, const unsigned char* __restrict__ Kc, const unsigned char* __restrict__ Vc,
             const unsigned char* __restrict__ wq, const unsigned char* __restrict__ wo, const unsigned char* __restrict__ w1,
             const unsigned char* __restrict__ w3, const unsigned char* __restrict__ w2, u16* __restrict__ out, u64* __restrict__ wsp) {
  __shared__ __attribute__((aligned(16))) float lds[16 * 192 + 96 + 32 + 320 + 648 + 64 + 4];
  float* l_o = lds;
  unsigned* l_q = (unsigned*)(lds + 3072);
  float* l_ml = lds + 3072 + 96;
  float* l_gv = lds + 3072 + 128;
  float* l_xn = lds + 3072 + 448;
  volatile LAS unsigned* l_f = (volatile LAS unsigned*)(lds + 3072 + 1096);
  volatile LAS unsigned* l_tag = (volatile LAS unsigned*)(lds + 3072 + 1160);

  const int b = blockIdx.x, tid = threadIdx.x, lane = tid & 63, w = __builtin_amdgcn_readfirstlane(tid >> 6);
  const int h = b >> 3, s = b & 7;
  const int tile = s * 30 + h;
  const int rgp = tile / 10, cc = tile - rgp * 10;
  gu64* ws = (gu64*)wsp;
  const size_t kvbase = ((size_t)h * SK + (SK - WIN) + s * 512 + w * 32) * ROWB;
  const __amdgpu_buffer_rsrc_t rk = __builtin_amdgcn_make_buffer_rsrc((void*)(Kc + kvbase), 0, 32 * ROWB, 0x00020000);
  const __amdgpu_buffer_rsrc_t rv = __builtin_amdgcn_make_buffer_rsrc((void*)(Vc + kvbase), 0, 32 * ROWB, 0x00020000);
  const int bit3 = (lane >> 3) & 1, rg = lane >> 3, c8 = lane & 7;
  const int offA = (lane >> 4) * ROWB + (lane & 15) * 16;
  const int offC = ((lane >> 4) + 4 * bit3) * ROWB + 256 + c8 * 16;

  __builtin_amdgcn_s_setprio(1);
  if (w < 6) {
    if (w == 3) {
      int vz = 0; PIN(vz);
      const u64 e0 = get_g(ws + WS_EPOCH + vz);
      const v4u xa = *(const v4u*)(xg + lane * 8);
      const unsigned xb = *(const unsigned*)(xg + 512 + lane * 2);
      l_f[lane] = 0u;
      SB();
      asm volatile("s_waitcnt lgkmcnt(0)\n\ts_barrier" ::: "memory");
      SB();
      float xv[10];
#pragma unroll
      for (int i = 0; i < 4; ++i) { xv[2 * i] = bf_lo(xa[i]); xv[2 * i + 1] = bf_hi(xa[i]); }
      xv[8] = bf_lo(xb); xv[9] = bf_hi(xb);
      float sm = 0.f;
#pragma unroll
      for (int i = 0; i < 10; ++i) sm += xv[i];
      const float mean = wave_sum(sm) * (1.0f / DM);
      float sq = 0.f;
#pragma unroll
      for (int i = 0; i < 10; ++i) { const float a = xv[i] - mean; sq = fmaf(a, a, sq); }
      const float var = wave_sum(sq) * (1.0f / DM);
      const float mb = rbf(mean), rs = 1.0f / rbf(sqrtf(rbf(rbf(var) + 1e-5f)));
#pragma unroll
      for (int i = 0; i < 10; ++i) xv[i] = rbf(rbf(xv[i] - mb) * rs);
#pragma unroll
      for (int i = 0; i < 4; ++i) *(LAS f2*)(l_xn + lane * 8 + 2 * i) = f2{xv[2 * i], xv[2 * i + 1]};
      *(LAS f2*)(l_xn + 512 + lane * 2) = f2{xv[8], xv[9]};
      const u64 e1 = next_epoch(e0);
      if (lane == 0) { l_tag[0] = (unsigned)e1; l_tag[1] = (unsigned)(e1 >> 32); }
      LDS_FENCE();
      l_f[F_X] = 1u;
    } else {
      const bool isq = w < 3;
      if (isq || b < 240) {
        const int nr = isq ? 10 : 8, row0 = isq ? 80 * s : 64 * cc, stride = isq ? 12288 : 3072;
        const unsigned char* wb = isq ? wq + h * 384 + w * 128 : (w == 4 ? w1 : w3) + rgp * 128;
        const __amdgpu_buffer_rsrc_t rw = __builtin_amdgcn_make_buffer_rsrc((void*)(wb + (size_t)row0 * stride), 0, (8 * nr - 1) * stride + 128, 0x00020000);
        const int woff = rg * nr * stride + c8 * 16, woff2 = isq ? woff : OOB;
        v4u wr[10];
#pragma unroll
        for (int i = 0; i < 10; ++i) { wr[i] = ld_nt(rw, i < 8 ? woff : woff2, i * stride); if (i == 1) { SB(); asm volatile("s_barrier" ::: "memory"); SB(); } }
        SB();
        wait_flags(l_f + F_X, 1, lane, 1);
        f2 xn[5];
#pragma unroll
        for (int i = 0; i < 5; ++i) xn[i] = *(const LAS f2*)(l_xn + row0 + nr * rg + 2 * i);
        if (!isq) xn[4] = f2{0.f, 0.f};
        const unsigned t0 = __builtin_amdgcn_readfirstlane(l_tag[0]), t1 = __builtin_amdgcn_readfirstlane(l_tag[1]);
        f2 acc[4];
#pragma unroll
        for (int j = 0; j < 4; ++j) acc[j] = f2{0.f, 0.f};
#pragma unroll
        for (int i = 0; i < 10; ++i) axpy(acc, xn[i >> 1][i & 1], wr[i]);
        gu64* dst = ws + (isq ? WS_QP + (size_t)((h * 3 + w) * 8 + s) * 64 : WS_GP + (size_t)tile * 128 + (w - 4) * 64);
        put_f(dst + 8 * c8 + rg, (t0 >> 8) | (t1 << 24), t0 & 0xffu, treduce8(acc, bit3));
        SB();
      } else {
        asm volatile("s_barrier" ::: "memory");
      }
    }
  } else {
    asm volatile("s_barrier" ::: "memory");
  }
  v4u kr[12], vr[12];
#pragma unroll
  for (int g = 0; g < 4; ++g) {
    kr[3 * g] = ld_nt(rk, offA, g * 3072);
    kr[3 * g + 1] = ld_nt(rk, offA, g * 3072 + 1536);
    kr[3 * g + 2] = ld_nt(rk, offC, g * 3072);
    SB();
  }
#pragma unroll
  for (int g = 0; g < 4; ++g) {
    vr[3 * g] = ld_nt(rv, offA, g * 3072);
    vr[3 * g + 1] = ld_nt(rv, offA, g * 3072 + 1536);
    vr[3 * g + 2] = ld_nt(rv, offC, g * 3072);
    SB();
  }
  wait_flags(l_f + F_X, 1, lane, 1);
  const unsigned e1lo = __builtin_amdgcn_readfirstlane(l_tag[0]), e1hi = __builtin_amdgcn_readfirstlane(l_tag[1]);
  const unsigned thi = (e1lo >> 8) | (e1hi << 24), tlo = e1lo & 0xffu;
  if (w < 3) {
    const gu64* qp = ws + WS_QP + (size_t)(h * 3 + w) * 512 + lane;
    u64 g[8];
#pragma nounroll
    for (unsigned spin = 0; spin < SPIN_CAP; ++spin) {
      bool ok = true;
#pragma unroll
      for (int p = 0; p < 8; ++p) g[p] = get_g(qp + p * 64);
#pragma unroll
      for (int p = 0; p < 8; ++p) ok = ok & g_ok(g[p], thi, tlo);
      if (__all(ok)) break;
      __builtin_amdgcn_s_sleep(8);
    }
    float t = 0.f;
#pragma unroll
    for (int p = 0; p < 8; ++p) t += g_val(g[p]);
    ((LAS __bf16*)l_q)[w * 64 + lane] = (__bf16)t;
    LDS_FENCE();
    l_f[F_Q + w] = 1u;
  }
  wait_flags(l_f + F_Q, 3, lane, 1);
  {
    const v4u qa = *(const LAS v4u*)(l_q + (lane & 15) * 4);
    const v4u qc = *(const LAS v4u*)(l_q + 64 + c8 * 4);
    float xs[4];
#pragma unroll
    for (int g = 0; g < 4; ++g) {
      float sa = 0.f, sb = 0.f, sc = 0.f;
#pragma unroll
      for (int k = 0; k < 4; ++k) { sa = dot2(kr[3 * g][k], qa[k], sa); sb = dot2(kr[3 * g + 1][k], qa[k], sb); sc = dot2(kr[3 * g + 2][k], qc[k], sc); }
      const float keep = bit3 ? sb : sa, send = bit3 ? sa : sb;
      xs[g] = keep + sc + dpp<ROR8>(send);
      PIN(xs[g]);
    }
    float x2[2];
#pragma unroll
    for (int j = 0; j < 2; ++j) { const float keep = (lane & 4) ? xs[j + 2] : xs[j], send = (lane & 4) ? xs[j] : xs[j + 2]; x2[j] = keep + dpp<XOR7>(send); }
    float sco = ((lane & 2) ? x2[1] : x2[0]) + dpp<XOR2>((lane & 2) ? x2[0] : x2[1]);
    sco += dpp<XOR1>(sco);
    sco = rbf(rbf(sco) * 0.07216878364870322f) * LOG2E;
    const float mw = wave_max(sco);
    float p = __builtin_amdgcn_exp2f(sco - mw);
    const float lw = wave_sum(p) * 0.5f;
    if (lane == 0) *(LAS f2*)(l_ml + 2 * w) = f2{mw, lw};
    LDS_ORDER();
    l_f[F_ST + w] = 1u;
    PIN(p);
    SB();
    f2 oa[4], oc[4];
#pragma unroll
    for (int j = 0; j < 4; ++j) { oa[j] = f2{0.f, 0.f}; oc[j] = f2{0.f, 0.f}; }
#define PV_G(G) { const float pa = dpp<BCAST + 2 * (G)>(p), pb = dpp<BCAST + 8 + 2 * (G)>(p); \
    axpy(oa, pa, vr[3 * (G)]); axpy(oa, pb, vr[3 * (G) + 1]); axpy(oc, bit3 ? pb : pa, vr[3 * (G) + 2]); SB(); }
    PV_G(0) PV_G(1) PV_G(2)
#undef PV_G
    float* lo = l_o + w * 192;
    LAS f2* loa = (LAS f2*)(lo + 8 * (lane & 15) + 2 * (lane >> 4));
    float* loc = lo + 128 + 8 * c8 + rg;
    const float pa = dpp<BCAST + 6>(p), pb = dpp<BCAST + 14>(p); float pc = bit3 ? pb : pa;
    PIN(pc);
    axpy(oa, pa, vr[9]); SB();
    axpy(oa, pb, vr[10]);
    *loa = f2{swap16_add(swap32_add(oa[0].x, oa[2].x), swap32_add(oa[1].x, oa[3].x)), swap16_add(swap32_add(oa[0].y, oa[2].y), swap32_add(oa[1].y, oa[3].y))};
    SB();
    axpy(oc, pc, vr[11]);
    *loc = treduce8(oc, bit3);
    LDS_ORDER();
    l_f[F_DONE + w] = 1u;
  }
  if (w >= 8) return;
  if (w < 3) {
    wait_flags(l_f + F_ST, 16, lane, 1);
    const f2 ml = *(const LAS f2*)(l_ml + 2 * (lane & 15));
    float M = ml.x;
    M = fmaxf(M, dpp<XOR1>(M)); M = fmaxf(M, dpp<XOR2>(M)); M = fmaxf(M, dpp<XOR7>(M)); M = fmaxf(M, dpp<ROR8>(M));
    const float sc = __builtin_amdgcn_exp2f(ml.x - M);
    if (w == 0) {
      float L = sc * ml.y;
      L += dpp<XOR1>(L); L += dpp<XOR2>(L); L += dpp<XOR7>(L); L += dpp<ROR8>(L);
      const unsigned bits = lane == 0 ? __float_as_uint(M) : lane == 1 ? (__float_as_uint(M) << 24) : __float_as_uint(L);
      if (lane < 3) put_bits(ws + WS_ST + (size_t)b * 4 + lane, thi, tlo, bits);
    }
    const int d = w * 64 + lane;
    gu64* dst = ws + WS_OP + (size_t)((h * 3 + w) * 8 + s) * 64 + lane;
    float o = 0.f;
#define CMB(i) o = fmaf(dpp<BCAST + (i)>(sc), l_o[(i) * 192 + d], o);
    wait_flags(l_f + F_DONE, 12, lane, 0);
    CMB(0) CMB(1) CMB(2) CMB(3) CMB(4) CMB(5) CMB(6) CMB(7) CMB(8) CMB(9) CMB(10) CMB(11)
#undef CMB
    float s12 = dpp<BCAST + 12>(sc), s13 = dpp<BCAST + 13>(sc), s14 = dpp<BCAST + 14>(sc), s15 = dpp<BCAST + 15>(sc);
    PIN(o); PIN(s12); PIN(s13); PIN(s14); PIN(s15);
    wait_flags(l_f + F_DONE + 12, 4, lane, 0);
    o = fmaf(s12, l_o[12 * 192 + d], o); o = fmaf(s13, l_o[13 * 192 + d], o); o = fmaf(s14, l_o[14 * 192 + d], o); o = fmaf(s15, l_o[15 * 192 + d], o);
    put_f(dst, thi, tlo, o);
    l_f[F_PUB + w] = 1u;
    return;
  }
  const int j5 = w >= 4 ? w - 4 : 4;
  if (b < 240) {
    float y;
    if (w == 3) {
      const gu64* gp = ws + WS_GP + (size_t)rgp * 1280 + lane;
      u64 g[20];
#pragma nounroll
      for (unsigned spin = 0; spin < SPIN_CAP; ++spin) {
        bool ok = true;
#pragma unroll
        for (int k = 0; k < 20; ++k) g[k] = get_g(gp + k * 64);
#pragma unroll
        for (int k = 0; k < 20; ++k) ok = ok & g_ok(g[k], thi, tlo);
        if (__all(ok)) break;
        __builtin_amdgcn_s_sleep(8);
      }
      float gt = 0.f, up = 0.f;
#pragma unroll
      for (int k = 0; k < 10; ++k) { gt += g_val(g[2 * k]); up += g_val(g[2 * k + 1]); }
      gt = rbf(gt); up = rbf(up);
      y = rbf(rbf(gt / (1.0f + __expf(-gt))) * up);
    }
    const unsigned char* wb = (w >= 4 ? wo + (size_t)(256 * rgp + 64 * j5) * 1280 : w2 + (size_t)(64 * rgp) * 1280) + cc * 128;
    const __amdgpu_buffer_rsrc_t rw = __builtin_amdgcn_make_buffer_rsrc((void*)wb, 0, 63 * 1280 + 128, 0x00020000);
    __builtin_amdgcn_s_setprio(0);
    v4u wr[8];
#pragma unroll
    for (int i = 0; i < 8; ++i) wr[i] = ld_nt(rw, rg * 8 * 1280 + c8 * 16, i * 1280);
    SB();
    __builtin_amdgcn_s_setprio(1);
    f2 wf[32];
    if (w >= 4) {
      const int beta = 4 * rgp + j5, hd = beta / 3;
      const gu64* op = ws + WS_OP + (size_t)beta * 512 + lane;
      const gu64* sp = ws + WS_ST + (size_t)hd * 32 + (lane & 28) + min(lane & 3, 2);
      wait_flags(l_f + F_PUB, 3, lane, 1);
      u64 g[8], gs;
      gs = get_g(sp);
#pragma unroll
      for (int p = 0; p < 8; ++p) g[p] = get_g(op + p * 64);
      SB();
#pragma unroll
      for (int i = 0; i < 8; ++i) PIN(wr[i]);
#pragma unroll
      for (int i = 0; i < 8; ++i)
#pragma unroll
        for (int k = 0; k < 4; ++k) { wf[4 * i + k] = f2{bf_lo(wr[i][k]), bf_hi(wr[i][k])}; PIN(wf[4 * i + k]); }
      SB();
#pragma nounroll
      for (unsigned spin = 0; spin < SPIN_CAP && !__all(g_ok(gs, thi, tlo)); ++spin) gs = get_g(sp);
      float cfs[8];
      {
        const unsigned lo = (unsigned)gs;
        const unsigned mh = __builtin_amdgcn_mov_dpp(lo, 0x00, 0xf, 0xf, true), ml = __builtin_amdgcn_mov_dpp(lo, 0x55, 0xf, 0xf, true), ll = __builtin_amdgcn_mov_dpp(lo, 0xAA, 0xf, 0xf, true);
        const float Mp = __uint_as_float((mh << 8) | ((ml >> 16) & 0xffu)), Lp = __uint_as_float(ll << 8);
        const float M = wave_max(Mp);
        float cf = __builtin_amdgcn_exp2f(Mp - M);
        float L = cf * Lp;
        L += dpp<XOR7>(L); L += dpp<ROR8>(L); L = swap16_add(L, L);
        cf = cf / L;
#pragma unroll
        for (int p = 0; p < 8; ++p) cfs[p] = __builtin_bit_cast(float, __builtin_amdgcn_readlane(__builtin_bit_cast(int, cf), 4 * p));
      }
#pragma nounroll
      for (unsigned spin = 0; spin < SPIN_CAP; ++spin) {
        bool ok[8], all = true;
#pragma unroll
        for (int p = 0; p < 8; ++p) { ok[p] = g_ok(g[p], thi, tlo); all = all & ok[p]; }
        if (__all(all)) break;
#pragma unroll
        for (int p = 0; p < 8; ++p) if (!__all(ok[p])) g[p] = get_g(op + p * 64);
      }
      y = ((cfs[0] * g_val(g[0]) + cfs[1] * g_val(g[1])) + (cfs[2] * g_val(g[2]) + cfs[3] * g_val(g[3]))) +
          ((cfs[4] * g_val(g[4]) + cfs[5] * g_val(g[5])) + (cfs[6] * g_val(g[6]) + cfs[7] * g_val(g[7])));
      y = rbf(y);
    } else {
#pragma unroll
      for (int i = 0; i < 8; ++i)
#pragma unroll
        for (int k = 0; k < 4; ++k) wf[4 * i + k] = f2{bf_lo(wr[i][k]), bf_hi(wr[i][k])};
    }
    f2 acc[4];
#pragma unroll
    for (int j = 0; j < 4; ++j) acc[j] = f2{0.f, 0.f};
#define GV(i) { const float y0 = dpp<BCAST + (i)>(y), y1 = dpp<BCAST + 8 + (i)>(y); const float yi = bit3 ? y1 : y0; \
    _Pragma("unroll") for (int k = 0; k < 4; ++k) acc[k] = __builtin_elementwise_fma(f2{yi, yi}, wf[4 * (i) + k], acc[k]); }
    GV(0) GV(1) GV(2) GV(3) GV(4) GV(5) GV(6) GV(7)
#undef GV
    l_gv[j5 * 64 + 8 * c8 + rg] = treduce8(acc, bit3);
    LDS_ORDER();
    unsigned old = 0;
    if (lane == 0) old = __hip_atomic_fetch_add((LAS unsigned*)(l_f + F_CNT), 1u, __ATOMIC_RELAXED, __HIP_MEMORY_SCOPE_WORKGROUP);
    LDS_ORDER();
    const float t = ((l_gv[lane] + l_gv[64 + lane]) + (l_gv[128 + lane] + l_gv[192 + lane])) + l_gv[256 + lane];
    if (__builtin_amdgcn_readfirstlane(old) == 4u)
      put_f(ws + WS_FP + ((size_t)(cc * 8 + rg) * 24 + rgp) * FP_PAD + c8, thi, tlo, t);
  } else {
    const int r = (b - 240) * 5 + j5;
    const float xres = __uint_as_float((unsigned)xg[r * 8 + c8] << 16);
    gu64* fp = ws + WS_FP + ((size_t)r * 24 + rg) * FP_PAD + c8;
    wait_flags(l_f + F_PUB, 3, lane, 1);
    u64 g[3] = {0, 0, 0};
#pragma nounroll
    for (unsigned spin = 0; spin < SPIN_CAP; ++spin) {
      bool ok = true;
#pragma unroll
      for (int k = 0; k < 3; ++k) if (spin == 0 || !g_ok(g[k], thi, tlo)) g[k] = get_rmw(fp + k * 8 * FP_PAD);
#pragma unroll
      for (int k = 0; k < 3; ++k) ok = ok & g_ok(g[k], thi, tlo);
      if (__all(ok)) break;
    }
    float t = (g_val(g[0]) + g_val(g[1])) + g_val(g[2]);
    t += dpp<ROR8>(t);
    t = swap16_add(t, t);
    t = swap32_add(t, t);
    t += xres;
    if (lane < 8) out[r * 8 + lane] = __builtin_bit_cast(u16, (__bf16)t);
    if (r == 0 && lane == 0) __hip_atomic_store(ws + WS_EPOCH, ((u64)e1hi << 32) | e1lo, RLX_AGENT);
  }
}

extern "C" void kernel_launch(void* const* d_in, const int* in_sizes, int n_in, void* d_out, int out_size, void* d_ws, size_t ws_size,
                              hipStream_t stream) {
  (void)in_sizes; (void)n_in; (void)out_size; (void)ws_size;
  fused_decode<<<dim3(NWG), dim3(NTHR), 0, stream>>>((const u16*)d_in[0], (const unsigned char*)d_in[1], (const unsigned char*)d_in[2],
                                                      (const unsigned char*)d_in[3], (const unsigned char*)d_in[4], (const unsigned char*)d_in[6],
                                                      (const unsigned char*)d_in[7], (const unsigned char*)d_in[8], (u16*)d_out, (u64*)d_ws);
}
```

```cpp
#include <hip/hip_runtime.h>
#include <hip/hip_bf16.h>

typedef unsigned v4u __attribute__((ext_vector_type(4)));
typedef float f2 __attribute__((ext_vector_type(2)));
typedef float f4 __attribute__((ext_vector_type(4)));
typedef unsigned long long u64;
typedef unsigned short u16;
typedef __attribute__((ext_vector_type(2))) __bf16 bf16x2;
#define GAS __attribute__((address_space(1)))
#define LAS __attribute__((address_space(3)))
typedef GAS u64 gu64;
#define RLX_AGENT __ATOMIC_RELAXED, __HIP_MEMORY_SCOPE_AGENT

#define QW 2
constexpr int DM = 640, SK = 32768, WIN = 4096;
constexpr int NWG = 256, NTHR = 1024;
constexpr int ROWB = 384;
constexpr int WS_QP = 0;
constexpr int WS_GP = WS_QP + NWG * 1024;
constexpr int WS_OP = WS_GP + 240 * 512;
constexpr int WS_ST = WS_OP + NWG * 768;
constexpr int WS_UEND = WS_ST + NWG * 16;
constexpr size_t WS_EPOCH = WS_UEND / 8;
constexpr size_t WS_FP = WS_EPOCH + 64;
constexpr size_t WS_AC = WS_FP + (size_t)24 * 640;
constexpr size_t WS_IF = WS_AC + (size_t)80 * 32;
constexpr unsigned SPIN_CAP = 1u << 18;
constexpr float LOG2E = 1.4426950408889634f;
constexpr int OOB = 0x7fffff00;

__device__ __forceinline__ float bf_lo(unsigned d) { return __uint_as_float(d << 16); }
__device__ __forceinline__ float bf_hi(unsigned d) { return __uint_as_float(d & 0xffff0000u); }
__device__ __forceinline__ float rbf(float x) { return (float)(__bf16)x; }
__device__ __forceinline__ float dot2(unsigned a, unsigned b, float acc) {
  return __builtin_amdgcn_fdot2_f32_bf16(__builtin_bit_cast(bf16x2, a), __builtin_bit_cast(bf16x2, b), acc, false);
}
template <int CTRL> __device__ __forceinline__ float dpp(float x) {
  return __builtin_bit_cast(float, __builtin_amdgcn_mov_dpp(__builtin_bit_cast(int, x), CTRL, 0xf, 0xf, true));
}
constexpr int XOR1 = 0xB1, XOR2 = 0x4E, XOR7 = 0x141, ROR8 = 0x128, BCAST = 0x150;
__device__ __forceinline__ float swap32_add(float a, float b) {
  auto r = __builtin_amdgcn_permlane32_swap(__float_as_uint(a), __float_as_uint(b), false, false);
  return __uint_as_float(r[0]) + __uint_as_float(r[1]);
}
__device__ __forceinline__ float swap16_add(float a, float b) {
  auto r = __builtin_amdgcn_permlane16_swap(__float_as_uint(a), __float_as_uint(b), false, false);
  return __uint_as_float(r[0]) + __uint_as_float(r[1]);
}
__device__ __forceinline__ float wave_sum(float x) {
  x += dpp<XOR1>(x); x += dpp<XOR2>(x); x += dpp<XOR7>(x); x += dpp<ROR8>(x);
  x = swap16_add(x, x); x = swap32_add(x, x);
  return x;
}
__device__ __forceinline__ float wave_max(float x) {
  x = fmaxf(x, dpp<XOR1>(x)); x = fmaxf(x, dpp<XOR2>(x)); x = fmaxf(x, dpp<XOR7>(x)); x = fmaxf(x, dpp<ROR8>(x));
  auto r = __builtin_amdgcn_permlane16_swap(__float_as_uint(x), __float_as_uint(x), false, false);
  x = fmaxf(__uint_as_float(r[0]), __uint_as_float(r[1]));
  auto t = __builtin_amdgcn_permlane32_swap(__float_as_uint(x), __float_as_uint(x), false, false);
  return fmaxf(__uint_as_float(t[0]), __uint_as_float(t[1]));
}
__device__ __forceinline__ float treduce8(const f2 (&a)[4], int bit3) {
  const float e = swap16_add(swap32_add(a[0].x, a[2].x), swap32_add(a[1].x, a[3].x));
  const float o = swap16_add(swap32_add(a[0].y, a[2].y), swap32_add(a[1].y, a[3].y));
  return (bit3 ? o : e) + dpp<ROR8>(bit3 ? e : o);
}
__device__ __forceinline__ void axpy(f2 (&acc)[4], float p, v4u v) {
#pragma unroll
  for (int k = 0; k < 4; ++k) acc[k] = __builtin_elementwise_fma(f2{p, p}, f2{bf_lo(v[k]), bf_hi(v[k])}, acc[k]);
}
__device__ __forceinline__ void put_bits(gu64* g, unsigned thi, unsigned tlo, unsigned bits) {
  const unsigned lo = __builtin_amdgcn_alignbit(tlo, bits, 8);
  __hip_atomic_store(g, ((u64)thi << 32) | lo, RLX_AGENT);
}
__device__ __forceinline__ void put_f(gu64* g, unsigned thi, unsigned tlo, float v) { put_bits(g, thi, tlo, __float_as_uint(v)); }
__device__ __forceinline__ u64 get_g(const gu64* g) { return __hip_atomic_load(const_cast<gu64*>(g), RLX_AGENT); }
__device__ __forceinline__ bool g_ok(u64 x, unsigned thi, unsigned tlo) { return ((unsigned)(x >> 32) == thi) & (((unsigned)x >> 24) == tlo); }
__device__ __forceinline__ float g_val(u64 x) { return __uint_as_float((unsigned)x << 8); }
__device__ __forceinline__ v4u unit_tags(unsigned e1lo) {
  const unsigned c = e1lo & 7u, s0 = ((e1lo >> 3) & 127u) << 3 | c;
  return v4u{s0, s0 ^ (8u | (((e1lo >> 10) & 63u) << 4)), ((e1lo >> 16) & 127u) << 3 | c, ((e1lo >> 23) & 127u) << 3 | c};
}
__device__ __forceinline__ void put_u(__amdgpu_buffer_rsrc_t r, int off, v4u tg, unsigned a, unsigned b, unsigned c, unsigned d) {
  const v4u u = {__builtin_amdgcn_alignbit(tg.x, a, 10), __builtin_amdgcn_alignbit(tg.y, b, 10), __builtin_amdgcn_alignbit(tg.z, c, 10), __builtin_amdgcn_alignbit(tg.w, d, 10)};
  __builtin_amdgcn_raw_buffer_store_b128(u, r, off, 0, 16);
}
__device__ __forceinline__ void put_u(__amdgpu_buffer_rsrc_t r, int off, v4u tg, f4 v) {
  put_u(r, off, tg, __float_as_uint(v.x) + 512u, __float_as_uint(v.y) + 512u, __float_as_uint(v.z) + 512u, __float_as_uint(v.w) + 512u);
}
__device__ __forceinline__ v4u get_u(__amdgpu_buffer_rsrc_t r, int off, int soff) {
  return __builtin_amdgcn_raw_buffer_load_b128(r, off, soff, 16);
}
__device__ __forceinline__ bool u_ok(v4u x, v4u tg) { return ((x.x >> 22) == tg.x) & ((x.y >> 22) == tg.y) & ((x.z >> 22) == tg.z) & ((x.w >> 22) == tg.w); }
__device__ __forceinline__ f4 u_val(v4u x) { return f4{__uint_as_float(x.x << 10), __uint_as_float(x.y << 10), __uint_as_float(x.z << 10), __uint_as_float(x.w << 10)}; }
__device__ __forceinline__ float treduce4(f4 v) { return swap16_add(swap32_add(v.x, v.z), swap32_add(v.y, v.w)); }
__device__ __forceinline__ u64 next_epoch(u64 e0) {
  const u64 e = ((u64)(unsigned)__builtin_amdgcn_readfirstlane((unsigned)(e0 >> 32)) << 32) | (unsigned)__builtin_amdgcn_readfirstlane((unsigned)e0);
  return e * 6364136223846793005ull + 1442695040888963407ull;
}
__device__ __forceinline__ v4u ld_nt(__amdgpu_buffer_rsrc_t r, int voff, int soff) { return __builtin_amdgcn_raw_buffer_load_b128(r, voff, soff, 19); }
#define SB() __builtin_amdgcn_sched_barrier(0)
#define PIN(x) asm volatile("" : "+v"(x))
#define LDS_ORDER() asm volatile("" ::: "memory")
#define LDS_FENCE() asm volatile("s_waitcnt lgkmcnt(0)" ::: "memory")

constexpr int F_DONE = 0, F_ST = 16, F_Q = 32, F_PUB = 36, F_X = 40, F_CNT = 41;

__device__ __forceinline__ void wait_flags(volatile LAS unsigned* f, int n, int lane, int sleep) {
  const int i = lane < n ? lane : 0;
  while (!__all(f[i] != 0u)) { if (sleep) __builtin_amdgcn_s_sleep(4); else __builtin_amdgcn_s_sleep(0); }
  asm volatile("" ::: "memory");
}

__global__ void __launch_bounds__(NTHR, 4)
fused_decode(const u16* __restrict__ xg, const unsigned char* __restrict__ Kc, const unsigned char* __restrict__ Vc,
             const unsigned char* __restrict__ wq, const unsigned char* __restrict__ wo, const unsigned char* __restrict__ w1,
             const unsigned char* __restrict__ w3, const unsigned char* __restrict__ w2, u16* __restrict__ out, u64* __restrict__ wsp) {
  __shared__ __attribute__((aligned(16))) float lds[16 * 192 + 96 + 32 + 320 + 648 + 64 + 4 + 320];
  float* l_o = lds;
  unsigned* l_q = (unsigned*)(lds + 3072);
  float* l_ml = lds + 3072 + 96;
  float* l_gv = lds + 3072 + 128;
  float* l_xn = lds + 3072 + 448;
  volatile LAS unsigned* l_f = (volatile LAS unsigned*)(lds + 3072 + 1096);
  volatile LAS unsigned* l_tag = (volatile LAS unsigned*)(lds + 3072 + 1160);
  unsigned* l_x = (unsigned*)(lds + 3072 + 1164);

  const int b = blockIdx.x, tid = threadIdx.x, lane = tid & 63, w = __builtin_amdgcn_readfirstlane(tid >> 6);
  const int h = b >> 3, s = b & 7;
  const int tile = s * 30 + h;
  const int rgp = tile / 10, cc = tile - rgp * 10;
  gu64* ws = (gu64*)wsp;
  const __amdgpu_buffer_rsrc_t ru = __builtin_amdgcn_make_buffer_rsrc((void*)wsp, 0, WS_UEND, 0x00020000);
  const size_t kvbase = ((size_t)h * SK + (SK - WIN) + s * 512 + w * 32) * ROWB;
  const __amdgpu_buffer_rsrc_t rk = __builtin_amdgcn_make_buffer_rsrc((void*)(Kc + kvbase), 0, 32 * ROWB, 0x00020000);
  const __amdgpu_buffer_rsrc_t rv = __builtin_amdgcn_make_buffer_rsrc((void*)(Vc + kvbase), 0, 32 * ROWB, 0x00020000);
  const int bit3 = (lane >> 3) & 1, rg = lane >> 3, c8 = lane & 7, r16 = lane >> 4, c16 = lane & 15;
  const int offA = (lane >> 4) * ROWB + (lane & 15) * 16;
  const int offC = ((lane >> 4) + 4 * bit3) * ROWB + 256 + c8 * 16;

  __builtin_amdgcn_s_setprio(1);
  if (w < 6) {
    if (w == 3) {
      int vz = 0; PIN(vz);
      const u64 e0 = get_g(ws + WS_EPOCH + vz);
      const v4u xa = *(const v4u*)(xg + lane * 8);
      const unsigned xb = *(const unsigned*)(xg + 512 + lane * 2);
      l_f[lane] = 0u;
      SB();
      asm volatile("s_waitcnt lgkmcnt(0)\n\ts_barrier" ::: "memory");
      SB();
      float xv[10];
#pragma unroll
      for (int i = 0; i < 4; ++i) { xv[2 * i] = bf_lo(xa[i]); xv[2 * i + 1] = bf_hi(xa[i]); }
      xv[8] = bf_lo(xb); xv[9] = bf_hi(xb);
      float sm = 0.f;
#pragma unroll
      for (int i = 0; i < 10; ++i) sm += xv[i];
      const float mean = wave_sum(sm) * (1.0f / DM);
      float sq = 0.f;
#pragma unroll
      for (int i = 0; i < 10; ++i) { const float a = xv[i] - mean; sq = fmaf(a, a, sq); }
      const float var = wave_sum(sq) * (1.0f / DM);
      const float mb = rbf(mean), rs = 1.0f / rbf(sqrtf(rbf(rbf(var) + 1e-5f)));
#pragma unroll
      for (int i = 0; i < 10; ++i) xv[i] = rbf(rbf(xv[i] - mb) * rs);
#pragma unroll
      for (int i = 0; i < 4; ++i) *(LAS f2*)(l_xn + lane * 8 + 2 * i) = f2{xv[2 * i], xv[2 * i + 1]};
      *(LAS f2*)(l_xn + 512 + lane * 2) = f2{xv[8], xv[9]};
      *(LAS v4u*)(l_x + lane * 4) = xa; l_x[256 + lane] = xb;
      const u64 e1 = next_epoch(e0);
      if (lane == 0) { l_tag[0] = (unsigned)e1; l_tag[1] = (unsigned)(e1 >> 32); }
      LDS_FENCE();
      l_f[F_X] = 1u;
    } else {
      const bool isq = w < 3;
      if (isq || b < 240) {
        const int nr = isq ? 10 : 8, row0 = isq ? 80 * s : 64 * cc, stride = isq ? 12288 : 3072;
        const unsigned char* wb = isq ? wq + h * 384 + w * 128 : (w == 4 ? w1 : w3) + rgp * 128;
        const __amdgpu_buffer_rsrc_t rw = __builtin_amdgcn_make_buffer_rsrc((void*)(wb + (size_t)row0 * stride), 0, (8 * nr - 1) * stride + 128, 0x00020000);
        const int woff = rg * nr * stride + c8 * 16, woff2 = isq ? woff : OOB;
        v4u wr[10];
#pragma unroll
        for (int i = 0; i < 10; ++i) { wr[i] = ld_nt(rw, i < 8 ? woff : woff2, i * stride); if (i == 1) { SB(); asm volatile("s_barrier" ::: "memory"); SB(); } }
        SB();
        wait_flags(l_f + F_X, 1, lane, 1);
        f2 xn[5];
#pragma unroll
        for (int i = 0; i < 5; ++i) xn[i] = *(const LAS f2*)(l_xn + row0 + nr * rg + 2 * i);
        if (!isq) xn[4] = f2{0.f, 0.f};
        const v4u tg = unit_tags(__builtin_amdgcn_readfirstlane(l_tag[0]));
        f2 acc[4];
#pragma unroll
        for (int j = 0; j < 4; ++j) acc[j] = f2{0.f, 0.f};
#pragma unroll
        for (int i = 0; i < 10; ++i) axpy(acc, xn[i >> 1][i & 1], wr[i]);
        f4 t = {swap32_add(acc[0].x, acc[2].x), swap32_add(acc[0].y, acc[2].y), swap32_add(acc[1].x, acc[3].x), swap32_add(acc[1].y, acc[3].y)};
#pragma unroll
        for (int j = 0; j < 4; ++j) { t[j] = swap16_add(t[j], t[j]); t[j] += dpp<ROR8>(t[j]); }
        const int dst = (isq ? WS_QP + b * 1024 + w * 256 : WS_GP + tile * 512 + (w - 4) * 256) + (2 * c8 + (lane >> 5)) * 16;
        put_u(ru, (lane & 24) ? OOB : dst, tg, t);
        SB();
      } else {
        asm volatile("s_barrier" ::: "memory");
        if (w == 4 && b < 250) {
          __hip_atomic_store(ws + WS_AC + (size_t)((b - 240) * 8 + rg) * 32 + c8, 0ull, RLX_AGENT);
          wait_flags(l_f + F_X, 1, lane, 1);
          const unsigned t0 = __builtin_amdgcn_readfirstlane(l_tag[0]), t1 = __builtin_amdgcn_readfirstlane(l_tag[1]);
          asm volatile("s_waitcnt vmcnt(0)" ::: "memory");
          if (lane == 0) put_bits(ws + WS_IF + (b - 240) * 16, (t0 >> 8) | (t1 << 24), t0 & 0xffu, 0u);
          SB();
        }
      }
    }
  } else {
    asm volatile("s_barrier" ::: "memory");
  }
  v4u kr[12], vr[12];
#pragma unroll
  for (int g = 0; g < 4; ++g) {
    kr[3 * g] = ld_nt(rk, offA, g * 3072);
    kr[3 * g + 1] = ld_nt(rk, offA, g * 3072 + 1536);
    kr[3 * g + 2] = ld_nt(rk, offC, g * 3072);
    SB();
  }
  if (w == QW) {
    const v4u tg = unit_tags(__builtin_amdgcn_readfirstlane(l_tag[0]));
    int qo = WS_QP + min(lane, 47) * 16;
    v4u g[8];
#pragma nounroll
    for (unsigned spin = 0; spin < SPIN_CAP; ++spin) {
      bool ok = true;
      PIN(qo);
#pragma unroll
      for (int p = 0; p < 8; ++p) g[p] = get_u(ru, qo, (h * 8 + p) * 1024);
#pragma unroll
      for (int p = 0; p < 8; ++p) ok = ok & u_ok(g[p], tg);
      if (__all(ok)) break;
      __builtin_amdgcn_s_sleep(8);
    }
    f4 t = u_val(g[0]);
#pragma unroll
    for (int p = 1; p < 8; ++p) t += u_val(g[p]);
    const bf16x2 q01 = {(__bf16)t.x, (__bf16)t.y}, q23 = {(__bf16)t.z, (__bf16)t.w};
    if (lane < 48) { l_q[2 * lane] = __builtin_bit_cast(unsigned, q01); l_q[2 * lane + 1] = __builtin_bit_cast(unsigned, q23); }
    LDS_FENCE();
    l_f[F_Q] = 1u;
    SB();
  }
#pragma unroll
  for (int g = 0; g < 4; ++g) {
    vr[3 * g] = ld_nt(rv, offA, g * 3072);
    vr[3 * g + 1] = ld_nt(rv, offA, g * 3072 + 1536);
    vr[3 * g + 2] = ld_nt(rv, offC, g * 3072);
    SB();
  }
  wait_flags(l_f + F_Q, 1, lane, 1);
  const unsigned e1lo = __builtin_amdgcn_readfirstlane(l_tag[0]), e1hi = __builtin_amdgcn_readfirstlane(l_tag[1]);
  const unsigned thi = (e1lo >> 8) | (e1hi << 24), tlo = e1lo & 0xffu;
  const v4u tg = unit_tags(e1lo);
  {
    const v4u qa = *(const LAS v4u*)(l_q + (lane & 15) * 4);
    const v4u qc = *(const LAS v4u*)(l_q + 64 + c8 * 4);
    float xs[4];
#pragma unroll
    for (int g = 0; g < 4; ++g) {
      float sa = 0.f, sb = 0.f, sc = 0.f;
#pragma unroll
      for (int k = 0; k < 4; ++k) { sa = dot2(kr[3 * g][k], qa[k], sa); sb = dot2(kr[3 * g + 1][k], qa[k], sb); sc = dot2(kr[3 * g + 2][k], qc[k], sc); }
      const float keep = bit3 ? sb : sa, send = bit3 ? sa : sb;
      xs[g] = keep + sc + dpp<ROR8>(send);
      PIN(xs[g]);
    }
    float x2[2];
#pragma unroll
    for (int j = 0; j < 2; ++j) { const float keep = (lane & 4) ? xs[j + 2] : xs[j], send = (lane & 4) ? xs[j] : xs[j + 2]; x2[j] = keep + dpp<XOR7>(send); }
    float sco = ((lane & 2) ? x2[1] : x2[0]) + dpp<XOR2>((lane & 2) ? x2[0] : x2[1]);
    sco += dpp<XOR1>(sco);
    sco = rbf(rbf(sco) * 0.07216878364870322f) * LOG2E;
    const float mw = wave_max(sco);
    float p = __builtin_amdgcn_exp2f(sco - mw);
    const float lw = wave_sum(p) * 0.5f;
    if (lane == 0) *(LAS f2*)(l_ml + 2 * w) = f2{mw, lw};
    LDS_ORDER();
    l_f[F_ST + w] = 1u;
    PIN(p);
    SB();
    f2 oa[4], oc[4];
#pragma unroll
    for (int j = 0; j < 4; ++j) { oa[j] = f2{0.f, 0.f}; oc[j] = f2{0.f, 0.f}; }
#define PV_G(G) { const float pa = dpp<BCAST + 2 * (G)>(p), pb = dpp<BCAST + 8 + 2 * (G)>(p); \
    axpy(oa, pa, vr[3 * (G)]); axpy(oa, pb, vr[3 * (G) + 1]); axpy(oc, bit3 ? pb : pa, vr[3 * (G) + 2]); SB(); }
    PV_G(0) PV_G(1) PV_G(2)
#undef PV_G
    float* lo = l_o + w * 192;
    LAS f2* loa = (LAS f2*)(lo + 8 * (lane & 15) + 2 * (lane >> 4));
    float* loc = lo + 128 + 8 * c8 + rg;
    const float pa = dpp<BCAST + 6>(p), pb = dpp<BCAST + 14>(p); float pc = bit3 ? pb : pa;
    PIN(pc);
    axpy(oa, pa, vr[9]); SB();
    axpy(oa, pb, vr[10]);
    *loa = f2{swap16_add(swap32_add(oa[0].x, oa[2].x), swap32_add(oa[1].x, oa[3].x)), swap16_add(swap32_add(oa[0].y, oa[2].y), swap32_add(oa[1].y, oa[3].y))};
    SB();
    axpy(oc, pc, vr[11]);
    *loc = treduce8(oc, bit3);
    LDS_ORDER();
    l_f[F_DONE + w] = 1u;
  }
  if (w >= 8) return;
  if (w < 3) {
    if (w == 2) return;
    wait_flags(l_f + F_ST, 16, lane, 1);
    const f2 ml = *(const LAS f2*)(l_ml + 2 * c16);
    float M = ml.x;
    M = fmaxf(M, dpp<XOR1>(M)); M = fmaxf(M, dpp<XOR2>(M)); M = fmaxf(M, dpp<XOR7>(M)); M = fmaxf(M, dpp<ROR8>(M));
    const float sc = __builtin_amdgcn_exp2f(ml.x - M);
    if (w == 1) {
      float L = sc * ml.y;
      L += dpp<XOR1>(L); L += dpp<XOR2>(L); L += dpp<XOR7>(L); L += dpp<ROR8>(L);
      put_u(ru, lane == 0 ? WS_ST + b * 16 : OOB, tg, __float_as_uint(M), __float_as_uint(M) << 22, __float_as_uint(L), __float_as_uint(L) << 22);
      return;
    }
    const LAS f4* lo = (const LAS f4*)l_o + min(lane, 47);
    const int dst = lane < 48 ? WS_OP + ((h * 3 + r16) * 8 + s) * 256 + c16 * 16 : OOB;
    f4 o = {0.f, 0.f, 0.f, 0.f};
#define CMB(i) o += dpp<BCAST + (i)>(sc) * lo[(i) * 48];
    wait_flags(l_f + F_DONE, 12, lane, 0);
    CMB(0) CMB(1) CMB(2) CMB(3) CMB(4) CMB(5) CMB(6) CMB(7) CMB(8) CMB(9) CMB(10) CMB(11)
#undef CMB
    float s12 = dpp<BCAST + 12>(sc), s13 = dpp<BCAST + 13>(sc), s14 = dpp<BCAST + 14>(sc), s15 = dpp<BCAST + 15>(sc);
    PIN(o); PIN(s12); PIN(s13); PIN(s14); PIN(s15);
    wait_flags(l_f + F_DONE + 12, 4, lane, 0);
    o += s12 * lo[12 * 48]; o += s13 * lo[13 * 48]; o += s14 * lo[14 * 48]; o += s15 * lo[15 * 48];
    put_u(ru, dst, tg, o);
    l_f[F_PUB] = 1u;
    return;
  }
  if (b >= 240) return;
  const int j5 = w >= 4 ? w - 4 : 4;
  {
    float y;
    if (w == 3) {
      int go = WS_GP + lane * 16;
      int vz = 0; PIN(vz);
      v4u g[5];
#pragma nounroll
      for (unsigned spin = 0; spin < SPIN_CAP; ++spin) {
        PIN(go);
#pragma unroll
        for (int k = 0; k < 5; ++k) g[k] = get_u(ru, go, rgp * 5120 + k * 1024);
        bool ok = g_ok(get_g(ws + WS_IF + cc * 16 + vz), thi, tlo);
#pragma unroll
        for (int k = 0; k < 5; ++k) ok = ok & u_ok(g[k], tg);
        if (__all(ok)) break;
        __builtin_amdgcn_s_sleep(8);
      }
      f4 t = ((u_val(g[0]) + u_val(g[1])) + (u_val(g[2]) + u_val(g[3]))) + u_val(g[4]);
      const f4 z = {0.f, 0.f, 0.f, 0.f};
      const float gt = rbf(treduce4((r16 & 1) ? z : t)), up = rbf(treduce4((r16 & 1) ? t : z));
      y = rbf(rbf(gt / (1.0f + __expf(-gt))) * up);
    }
    const unsigned char* wb = (w >= 4 ? wo + (size_t)(256 * rgp + 64 * j5) * 1280 : w2 + (size_t)(64 * rgp) * 1280) + cc * 128;
    const __amdgpu_buffer_rsrc_t rw = __builtin_amdgcn_make_buffer_rsrc((void*)wb, 0, 63 * 1280 + 128, 0x00020000);
    __builtin_amdgcn_s_setprio(0);
    v4u wr[8];
#pragma unroll
    for (int i = 0; i < 8; ++i) wr[i] = ld_nt(rw, (32 * bit3 + r16) * 1280 + c8 * 16, i * 4 * 1280);
    SB();
    __builtin_amdgcn_s_setprio(1);
    f2 wf[32];
    if (w >= 4) {
      const int beta = 4 * rgp + j5, hd = beta / 3;
      int oo = WS_OP + lane * 16, so = WS_ST + r16 * 16;
      wait_flags(l_f + F_PUB, 1, lane, 1);
      v4u g[2], gs[2];
      PIN(oo); PIN(so);
#pragma unroll
      for (int k = 0; k < 2; ++k) gs[k] = get_u(ru, so, hd * 128 + k * 64);
#pragma unroll
      for (int k = 0; k < 2; ++k) g[k] = get_u(ru, oo, beta * 2048 + k * 1024);
      SB();
#pragma unroll
      for (int i = 0; i < 8; ++i) PIN(wr[i]);
#pragma unroll
      for (int i = 0; i < 8; ++i)
#pragma unroll
        for (int k = 0; k < 4; ++k) { wf[4 * i + k] = f2{bf_lo(wr[i][k]), bf_hi(wr[i][k])}; PIN(wf[4 * i + k]); }
      SB();
#pragma nounroll
      for (unsigned spin = 0; spin < SPIN_CAP && !__all(u_ok(gs[0], tg) & u_ok(gs[1], tg)); ++spin) {
        PIN(so);
#pragma unroll
        for (int k = 0; k < 2; ++k) gs[k] = get_u(ru, so, hd * 128 + k * 64);
      }
      const float M0 = __uint_as_float((gs[0].x << 10) | ((gs[0].y >> 12) & 1023u)), M1 = __uint_as_float((gs[1].x << 10) | ((gs[1].y >> 12) & 1023u));
      const float L0 = __uint_as_float((gs[0].z << 10) | ((gs[0].w >> 12) & 1023u)), L1 = __uint_as_float((gs[1].z << 10) | ((gs[1].w >> 12) & 1023u));
      const float M = wave_max(fmaxf(M0, M1));
      float c0 = __builtin_amdgcn_exp2f(M0 - M), c1 = __builtin_amdgcn_exp2f(M1 - M);
      float L = c0 * L0 + c1 * L1;
      L = swap16_add(L, L); L = swap32_add(L, L);
      L = 1.0f / L;
      c0 *= L; c1 *= L;
      PIN(c0); PIN(c1);
#pragma nounroll
      for (unsigned spin = 0; spin < SPIN_CAP; ++spin) {
        const bool k0 = u_ok(g[0], tg), k1 = u_ok(g[1], tg);
        if (__all(k0 & k1)) break;
        PIN(oo);
        const v4u n0 = get_u(ru, k0 ? OOB : oo, beta * 2048), n1 = get_u(ru, k1 ? OOB : oo, beta * 2048 + 1024);
        g[0] = k0 ? g[0] : n0; g[1] = k1 ? g[1] : n1;
      }
      y = rbf(treduce4(c0 * u_val(g[0]) + c1 * u_val(g[1])));
    } else {
#pragma unroll
      for (int i = 0; i < 8; ++i)
#pragma unroll
        for (int k = 0; k < 4; ++k) wf[4 * i + k] = f2{bf_lo(wr[i][k]), bf_hi(wr[i][k])};
    }
    f2 acc[4];
#pragma unroll
    for (int j = 0; j < 4; ++j) acc[j] = f2{0.f, 0.f};
#define GV(i) { const float y0 = dpp<BCAST + (i)>(y), y1 = dpp<BCAST + 8 + (i)>(y); const float yi = bit3 ? y1 : y0; \
    _Pragma("unroll") for (int k = 0; k < 4; ++k) acc[k] = __builtin_elementwise_fma(f2{yi, yi}, wf[4 * (i) + k], acc[k]); }
    GV(0) GV(1) GV(2) GV(3) GV(4) GV(5) GV(6) GV(7)
#undef GV
    l_gv[j5 * 64 + 8 * c8 + rg] = treduce8(acc, bit3);
    LDS_ORDER();
    unsigned old = 0;
    if (lane == 0) old = __hip_atomic_fetch_add((LAS unsigned*)(l_f + F_CNT), 1u, __ATOMIC_RELAXED, __HIP_MEMORY_SCOPE_WORKGROUP);
    LDS_ORDER();
    const float t = ((l_gv[lane] + l_gv[64 + lane]) + (l_gv[128 + lane] + l_gv[192 + lane])) + l_gv[256 + lane];
    const float xres = __uint_as_float((unsigned)((const LAS u16*)l_x)[cc * 64 + lane] << 16);
    if (__builtin_amdgcn_readfirstlane(old) != 4u) return;
    const int col = cc * 64 + lane;
    const bool bad = !(fabsf(t) < 32768.0f) || wave_max(fabsf(t)) < 0.000244140625f;
    if (__builtin_expect(__any(bad), 0)) {
      if (bad) put_f(ws + WS_FP + (size_t)rgp * 640 + col, thi, tlo, t);
      asm volatile("s_waitcnt vmcnt(0)" ::: "memory");
    }
    const float fl = floorf(t);
    const u64 fix = ((u64)(unsigned)(int)fl << 32) | (unsigned)fminf((t - fl) * 4294967296.0f, 4294967040.0f);
    const u64 add = bad ? 65ull : (fix << 11) | 1ull;
    const u64 tot = __hip_atomic_fetch_add(ws + WS_AC + (size_t)(cc * 8 + rg) * 32 + c8, add, RLX_AGENT) + add;
    if (((unsigned)tot & 63u) == 24u) {
      float r = (float)((double)((long long)tot >> 11) * 2.3283064365386963e-10);
      const unsigned nb = ((unsigned)tot >> 6) & 31u;
      if (__builtin_expect(nb != 0u, 0)) {
        float e = 0.f;
#pragma nounroll
        for (unsigned spin = 0; spin < SPIN_CAP; ++spin) {
          unsigned n = 0; e = 0.f;
#pragma nounroll
          for (int k = 0; k < 24; ++k) { const u64 v = get_g(ws + WS_FP + (size_t)k * 640 + col); if (g_ok(v, thi, tlo)) { e += g_val(v); ++n; } }
          if (n == nb) break;
        }
        r += e;
      }
      out[col] = __builtin_bit_cast(u16, (__bf16)(r + xres));
      if (col == 0) __hip_atomic_store(ws + WS_EPOCH, ((u64)e1hi << 32) | e1lo, RLX_AGENT);
    }
  }
}

extern "C" void kernel_launch(void* const* d_in, const int* in_sizes, int n_in, void* d_out, int out_size, void* d_ws, size_t ws_size,
                              hipStream_t stream) {
  (void)in_sizes; (void)n_in; (void)out_size; (void)ws_size;
  fused_decode<<<dim3(NWG), dim3(NTHR), 0, stream>>>((const u16*)d_in[0], (const unsigned char*)d_in[1], (const unsigned char*)d_in[2],
                                                      (const unsigned char*)d_in[3], (const unsigned char*)d_in[4], (const unsigned char*)d_in[6],
                                                      (const unsigned char*)d_in[7], (const unsigned char*)d_in[8], (u16*)d_out, (u64*)d_ws);
}
```

```cpp
#include <hip/hip_runtime.h>
#include <hip/hip_bf16.h>

typedef unsigned v4u __attribute__((ext_vector_type(4)));
typedef float f2 __attribute__((ext_vector_type(2)));
typedef float f4 __attribute__((ext_vector_type(4)));
typedef unsigned long long u64;
typedef unsigned short u16;
typedef __attribute__((ext_vector_type(2))) __bf16 bf16x2;
#define GAS __attribute__((address_space(1)))
#define LAS __attribute__((address_space(3)))
typedef GAS u64 gu64;
#define RLX_AGENT __ATOMIC_RELAXED, __HIP_MEMORY_SCOPE_AGENT

constexpr int DM = 640, SK = 32768, WIN = 4096;
constexpr int NWG = 256, NTHR = 1024;
constexpr int ROWB = 384;
constexpr int WS_QP = 0;
constexpr int WS_GP = WS_QP + NWG * 1024;
constexpr int WS_OP = WS_GP + 240 * 512;
constexpr int WS_ST = WS_OP + NWG * 768;
constexpr int WS_UEND = WS_ST + NWG * 16;
constexpr size_t WS_EPOCH = WS_UEND / 8;
constexpr size_t WS_FP = WS_EPOCH + 64;
constexpr size_t WS_AC = WS_FP + (size_t)24 * 640;
constexpr size_t WS_IF = WS_AC + (size_t)80 * 32;
constexpr unsigned SPIN_CAP = 1u << 18;
constexpr float LOG2E = 1.4426950408889634f;
constexpr int OOB = 0x7fffff00;

__device__ __forceinline__ float bf_lo(unsigned d) { return __uint_as_float(d << 16); }
__device__ __forceinline__ float bf_hi(unsigned d) { return __uint_as_float(d & 0xffff0000u); }
__device__ __forceinline__ float rbf(float x) { return (float)(__bf16)x; }
__device__ __forceinline__ float dot2(unsigned a, unsigned b, float acc) {
  return __builtin_amdgcn_fdot2_f32_bf16(__builtin_bit_cast(bf16x2, a), __builtin_bit_cast(bf16x2, b), acc, false);
}
template <int CTRL> __device__ __forceinline__ float dpp(float x) {
  return __builtin_bit_cast(float, __builtin_amdgcn_mov_dpp(__builtin_bit_cast(int, x), CTRL, 0xf, 0xf, true));
}
constexpr int XOR1 = 0xB1, XOR2 = 0x4E, XOR7 = 0x141, ROR8 = 0x128, BCAST = 0x150;
__device__ __forceinline__ float swap32_add(float a, float b) {
  auto r = __builtin_amdgcn_permlane32_swap(__float_as_uint(a), __float_as_uint(b), false, false);
  return __uint_as_float(r[0]) + __uint_as_float(r[1]);
}
__device__ __forceinline__ float swap16_add(float a, float b) {
  auto r = __builtin_amdgcn_permlane16_swap(__float_as_uint(a), __float_as_uint(b), false, false);
  return __uint_as_float(r[0]) + __uint_as_float(r[1]);
}
__device__ __forceinline__ float wave_sum(float x) {
  x += dpp<XOR1>(x); x += dpp<XOR2>(x); x += dpp<XOR7>(x); x += dpp<ROR8>(x);
  x = swap16_add(x, x); x = swap32_add(x, x);
  return x;
}
__device__ __forceinline__ float wave_max(float x) {
  x = fmaxf(x, dpp<XOR1>(x)); x = fmaxf(x, dpp<XOR2>(x)); x = fmaxf(x, dpp<XOR7>(x)); x = fmaxf(x, dpp<ROR8>(x));
  auto r = __builtin_amdgcn_permlane16_swap(__float_as_uint(x), __float_as_uint(x), false, false);
  x = fmaxf(__uint_as_float(r[0]), __uint_as_float(r[1]));
  auto t = __builtin_amdgcn_permlane32_swap(__float_as_uint(x), __float_as_uint(x), false, false);
  return fmaxf(__uint_as_float(t[0]), __uint_as_float(t[1]));
}
__device__ __forceinline__ float treduce8(const f2 (&a)[4], int bit3) {
  const float e = swap16_add(swap32_add(a[0].x, a[2].x), swap32_add(a[1].x, a[3].x));
  const float o = swap16_add(swap32_add(a[0].y, a[2].y), swap32_add(a[1].y, a[3].y));
  return (bit3 ? o : e) + dpp<ROR8>(bit3 ? e : o);
}
__device__ __forceinline__ void axpy(f2 (&acc)[4], float p, v4u v) {
#pragma unroll
  for (int k = 0; k < 4; ++k) acc[k] = __builtin_elementwise_fma(f2{p, p}, f2{bf_lo(v[k]), bf_hi(v[k])}, acc[k]);
}
__device__ __forceinline__ void put_bits(gu64* g, unsigned thi, unsigned tlo, unsigned bits) {
  const unsigned lo = __builtin_amdgcn_alignbit(tlo, bits, 8);
  __hip_atomic_store(g, ((u64)thi << 32) | lo, RLX_AGENT);
}
__device__ __forceinline__ void put_f(gu64* g, unsigned thi, unsigned tlo, float v) { put_bits(g, thi, tlo, __float_as_uint(v)); }
__device__ __forceinline__ u64 get_g(const gu64* g) { return __hip_atomic_load(const_cast<gu64*>(g), RLX_AGENT); }
__device__ __forceinline__ bool g_ok(u64 x, unsigned thi, unsigned tlo) { return ((unsigned)(x >> 32) == thi) & (((unsigned)x >> 24) == tlo); }
__device__ __forceinline__ float g_val(u64 x) { return __uint_as_float((unsigned)x << 8); }
__device__ __forceinline__ v4u unit_tags(unsigned e1lo) {
  const unsigned c = e1lo & 7u, s0 = ((e1lo >> 3) & 127u) << 3 | c;
  return v4u{s0, s0 ^ (8u | (((e1lo >> 10) & 63u) << 4)), ((e1lo >> 16) & 127u) << 3 | c, ((e1lo >> 23) & 127u) << 3 | c};
}
__device__ __forceinline__ void put_u(__amdgpu_buffer_rsrc_t r, int off, v4u tg, unsigned a, unsigned b, unsigned c, unsigned d) {
  const v4u u = {__builtin_amdgcn_alignbit(tg.x, a, 10), __builtin_amdgcn_alignbit(tg.y, b, 10), __builtin_amdgcn_alignbit(tg.z, c, 10), __builtin_amdgcn_alignbit(tg.w, d, 10)};
  __builtin_amdgcn_raw_buffer_store_b128(u, r, off, 0, 16);
}
__device__ __forceinline__ void put_u(__amdgpu_buffer_rsrc_t r, int off, v4u tg, f4 v) {
  put_u(r, off, tg, __float_as_uint(v.x) + 512u, __float_as_uint(v.y) + 512u, __float_as_uint(v.z) + 512u, __float_as_uint(v.w) + 512u);
}
__device__ __forceinline__ v4u get_u(__amdgpu_buffer_rsrc_t r, int off, int soff) {
  return __builtin_amdgcn_raw_buffer_load_b128(r, off, soff, 16);
}
__device__ __forceinline__ bool u_ok(v4u x, v4u tg) { return ((x.x >> 22) == tg.x) & ((x.y >> 22) == tg.y) & ((x.z >> 22) == tg.z) & ((x.w >> 22) == tg.w); }
__device__ __forceinline__ f4 u_val(v4u x) { return f4{__uint_as_float(x.x << 10), __uint_as_float(x.y << 10), __uint_as_float(x.z << 10), __uint_as_float(x.w << 10)}; }
__device__ __forceinline__ float treduce4(f4 v) { return swap16_add(swap32_add(v.x, v.z), swap32_add(v.y, v.w)); }
__device__ __forceinline__ u64 next_epoch(u64 e0) {
  const u64 e = ((u64)(unsigned)__builtin_amdgcn_readfirstlane((unsigned)(e0 >> 32)) << 32) | (unsigned)__builtin_amdgcn_readfirstlane((unsigned)e0);
  return e * 6364136223846793005ull + 1442695040888963407ull;
}
__device__ __forceinline__ v4u ld_nt(__amdgpu_buffer_rsrc_t r, int voff, int soff) { return __builtin_amdgcn_raw_buffer_load_b128(r, voff, soff, 19); }
#define SB() __builtin_amdgcn_sched_barrier(0)
#define PIN(x) asm volatile("" : "+v"(x))
#define LDS_ORDER() asm volatile("" ::: "memory")
#define LDS_FENCE() asm volatile("s_waitcnt lgkmcnt(0)" ::: "memory")

constexpr int F_DONE = 0, F_ST = 16, F_Q = 32, F_PUB = 36, F_X = 40, F_CNT = 41;

__device__ __forceinline__ void wait_flags(volatile LAS unsigned* f, int n, int lane, int sleep) {
  const int i = lane < n ? lane : 0;
  while (!__all(f[i] != 0u)) { if (sleep) __builtin_amdgcn_s_sleep(4); else __builtin_amdgcn_s_sleep(0); }
  asm volatile("" ::: "memory");
}

__global__ void __launch_bounds__(NTHR, 4)
fused_decode(const u16* __restrict__ xg, const unsigned char* __restrict__ Kc, const unsigned char* __restrict__ Vc,
             const unsigned char* __restrict__ wq, const unsigned char* __restrict__ wo, const unsigned char* __restrict__ w1,
             const unsigned char* __restrict__ w3, const unsigned char* __restrict__ w2, u16* __restrict__ out, u64* __restrict__ wsp) {
  __shared__ __attribute__((aligned(16))) float lds[16 * 192 + 96 + 32 + 320 + 648 + 64 + 4 + 320];
  float* l_o = lds;
  unsigned* l_q = (unsigned*)(lds + 3072);
  float* l_ml = lds + 3072 + 96;
  float* l_gv = lds + 3072 + 128;
  float* l_xn = lds + 3072 + 448;
  volatile LAS unsigned* l_f = (volatile LAS unsigned*)(lds + 3072 + 1096);
  volatile LAS unsigned* l_tag = (volatile LAS unsigned*)(lds + 3072 + 1160);
  unsigned* l_x = (unsigned*)(lds + 3072 + 1164);

  const int b = blockIdx.x, tid = threadIdx.x, lane = tid & 63, w = __builtin_amdgcn_readfirstlane(tid >> 6);
  const int h = b >> 3, s = b & 7;
  const int tile = s * 30 + h;
  const int rgp = tile / 10, cc = tile - rgp * 10;
  gu64* ws = (gu64*)wsp;
  const __amdgpu_buffer_rsrc_t ru = __builtin_amdgcn_make_buffer_rsrc((void*)wsp, 0, WS_UEND, 0x00020000);
  const size_t kvbase = ((size_t)h * SK + (SK - WIN) + s * 512 + w * 32) * ROWB;
  const __amdgpu_buffer_rsrc_t rk = __builtin_amdgcn_make_buffer_rsrc((void*)(Kc + kvbase), 0, 32 * ROWB, 0x00020000);
  const __amdgpu_buffer_rsrc_t rv = __builtin_amdgcn_make_buffer_rsrc((void*)(Vc + kvbase), 0, 32 * ROWB, 0x00020000);
  const int bit3 = (lane >> 3) & 1, rg = lane >> 3, c8 = lane & 7, r16 = lane >> 4, c16 = lane & 15;
  const int offA = (lane >> 4) * ROWB + (lane & 15) * 16;
  const int offC = ((lane >> 4) + 4 * bit3) * ROWB + 256 + c8 * 16;

  __builtin_amdgcn_s_setprio(1);
  if (w < 6) {
    if (w == 3) {
      int vz = 0; PIN(vz);
      const u64 e0 = get_g(ws + WS_EPOCH + vz);
      const v4u xa = *(const v4u*)(xg + lane * 8);
      const unsigned xb = *(const unsigned*)(xg + 512 + lane * 2);
      l_f[lane] = 0u;
      SB();
      asm volatile("s_waitcnt lgkmcnt(0)\n\ts_barrier" ::: "memory");
      SB();
      float xv[10];
#pragma unroll
      for (int i = 0; i < 4; ++i) { xv[2 * i] = bf_lo(xa[i]); xv[2 * i + 1] = bf_hi(xa[i]); }
      xv[8] = bf_lo(xb); xv[9] = bf_hi(xb);
      float sm = 0.f;
#pragma unroll
      for (int i = 0; i < 10; ++i) sm += xv[i];
      const float mean = wave_sum(sm) * (1.0f / DM);
      float sq = 0.f;
#pragma unroll
      for (int i = 0; i < 10; ++i) { const float a = xv[i] - mean; sq = fmaf(a, a, sq); }
      const float var = wave_sum(sq) * (1.0f / DM);
      const float mb = rbf(mean), rs = 1.0f / rbf(sqrtf(rbf(rbf(var) + 1e-5f)));
#pragma unroll
      for (int i = 0; i < 10; ++i) xv[i] = rbf(rbf(xv[i] - mb) * rs);
#pragma unroll
      for (int i = 0; i < 4; ++i) *(LAS f2*)(l_xn + lane * 8 + 2 * i) = f2{xv[2 * i], xv[2 * i + 1]};
      *(LAS f2*)(l_xn + 512 + lane * 2) = f2{xv[8], xv[9]};
      *(LAS v4u*)(l_x + lane * 4) = xa; l_x[256 + lane] = xb;
      const u64 e1 = next_epoch(e0);
      if (lane == 0) { l_tag[0] = (unsigned)e1; l_tag[1] = (unsigned)(e1 >> 32); }
      LDS_FENCE();
      l_f[F_X] = 1u;
    } else {
      const bool isq = w < 3;
      if (isq || b < 240) {
        const int nr = isq ? 10 : 8, row0 = isq ? 80 * s : 64 * cc, stride = isq ? 12288 : 3072;
        const unsigned char* wb = isq ? wq + h * 384 + w * 128 : (w == 4 ? w1 : w3) + rgp * 128;
        const __amdgpu_buffer_rsrc_t rw = __builtin_amdgcn_make_buffer_rsrc((void*)(wb + (size_t)row0 * stride), 0, (8 * nr - 1) * stride + 128, 0x00020000);
        const int woff = rg * nr * stride + c8 * 16, woff2 = isq ? woff : OOB;
        v4u wr[10];
#pragma unroll
        for (int i = 0; i < 10; ++i) { wr[i] = ld_nt(rw, i < 8 ? woff : woff2, i * stride); if (i == 1) { SB(); asm volatile("s_barrier" ::: "memory"); SB(); } }
        SB();
        wait_flags(l_f + F_X, 1, lane, 1);
        f2 xn[5];
#pragma unroll
        for (int i = 0; i < 5; ++i) xn[i] = *(const LAS f2*)(l_xn + row0 + nr * rg + 2 * i);
        if (!isq) xn[4] = f2{0.f, 0.f};
        const v4u tg = unit_tags(__builtin_amdgcn_readfirstlane(l_tag[0]));
        f2 acc[4];
#pragma unroll
        for (int j = 0; j < 4; ++j) acc[j] = f2{0.f, 0.f};
#pragma unroll
        for (int i = 0; i < 10; ++i) axpy(acc, xn[i >> 1][i & 1], wr[i]);
        f4 t = {swap32_add(acc[0].x, acc[2].x), swap32_add(acc[0].y, acc[2].y), swap32_add(acc[1].x, acc[3].x), swap32_add(acc[1].y, acc[3].y)};
#pragma unroll
        for (int j = 0; j < 4; ++j) { t[j] = swap16_add(t[j], t[j]); t[j] += dpp<ROR8>(t[j]); }
        const int dst = (isq ? WS_QP + b * 1024 + w * 256 : WS_GP + tile * 512 + (w - 4) * 256) + (2 * c8 + (lane >> 5)) * 16;
        put_u(ru, (lane & 24) ? OOB : dst, tg, t);
        SB();
      } else {
        asm volatile("s_barrier" ::: "memory");
        if (w == 4 && b < 250) {
          __hip_atomic_store(ws + WS_AC + (size_t)((b - 240) * 8 + rg) * 32 + c8, 0ull, RLX_AGENT);
          wait_flags(l_f + F_X, 1, lane, 1);
          const unsigned t0 = __builtin_amdgcn_readfirstlane(l_tag[0]), t1 = __builtin_amdgcn_readfirstlane(l_tag[1]);
          asm volatile("s_waitcnt vmcnt(0)" ::: "memory");
          if (lane == 0) put_bits(ws + WS_IF + (b - 240) * 16, (t0 >> 8) | (t1 << 24), t0 & 0xffu, 0u);
          SB();
        }
      }
    }
  } else {
    asm volatile("s_barrier" ::: "memory");
  }
  v4u kr[12], vr[12];
#pragma unroll
  for (int g = 0; g < 4; ++g) {
    kr[3 * g] = ld_nt(rk, offA, g * 3072);
    kr[3 * g + 1] = ld_nt(rk, offA, g * 3072 + 1536);
    kr[3 * g + 2] = ld_nt(rk, offC, g * 3072);
    SB();
  }
  if (w == 2) {
    const v4u tg = unit_tags(__builtin_amdgcn_readfirstlane(l_tag[0]));
    int qo = WS_QP + min(lane, 47) * 16;
    asm volatile("s_waitcnt vmcnt(0)" ::: "memory");
    v4u g[8];
#pragma nounroll
    for (unsigned spin = 0; spin < SPIN_CAP; ++spin) {
      bool ok = true;
      PIN(qo);
#pragma unroll
      for (int p = 0; p < 8; ++p) g[p] = get_u(ru, qo, (h * 8 + p) * 1024);
#pragma unroll
      for (int p = 0; p < 8; ++p) ok = ok & u_ok(g[p], tg);
      if (__all(ok)) break;
      __builtin_amdgcn_s_sleep(8);
    }
    f4 t = u_val(g[0]);
#pragma unroll
    for (int p = 1; p < 8; ++p) t += u_val(g[p]);
    const bf16x2 q01 = {(__bf16)t.x, (__bf16)t.y}, q23 = {(__bf16)t.z, (__bf16)t.w};
    if (lane < 48) { l_q[2 * lane] = __builtin_bit_cast(unsigned, q01); l_q[2 * lane + 1] = __builtin_bit_cast(unsigned, q23); }
    LDS_FENCE();
    l_f[F_Q] = 1u;
    SB();
  }
#pragma unroll
  for (int g = 0; g < 4; ++g) {
    vr[3 * g] = ld_nt(rv, offA, g * 3072);
    vr[3 * g + 1] = ld_nt(rv, offA, g * 3072 + 1536);
    vr[3 * g + 2] = ld_nt(rv, offC, g * 3072);
    SB();
  }
  wait_flags(l_f + F_Q, 1, lane, 1);
  const unsigned e1lo = __builtin_amdgcn_readfirstlane(l_tag[0]), e1hi = __builtin_amdgcn_readfirstlane(l_tag[1]);
  const unsigned thi = (e1lo >> 8) | (e1hi << 24), tlo = e1lo & 0xffu;
  const v4u tg = unit_tags(e1lo);
  {
    const v4u qa = *(const LAS v4u*)(l_q + (lane & 15) * 4);
    const v4u qc = *(const LAS v4u*)(l_q + 64 + c8 * 4);
    float xs[4];
#pragma unroll
    for (int g = 0; g < 4; ++g) {
      float sa = 0.f, sb = 0.f, sc = 0.f;
#pragma unroll
      for (int k = 0; k < 4; ++k) { sa = dot2(kr[3 * g][k], qa[k], sa); sb = dot2(kr[3 * g + 1][k], qa[k], sb); sc = dot2(kr[3 * g + 2][k], qc[k], sc); }
      const float keep = bit3 ? sb : sa, send = bit3 ? sa : sb;
      xs[g] = keep + sc + dpp<ROR8>(send);
      PIN(xs[g]);
    }
    float x2[2];
#pragma unroll
    for (int j = 0; j < 2; ++j) { const float keep = (lane & 4) ? xs[j + 2] : xs[j], send = (lane & 4) ? xs[j] : xs[j + 2]; x2[j] = keep + dpp<XOR7>(send); }
    float sco = ((lane & 2) ? x2[1] : x2[0]) + dpp<XOR2>((lane & 2) ? x2[0] : x2[1]);
    sco += dpp<XOR1>(sco);
    sco = rbf(rbf(sco) * 0.07216878364870322f) * LOG2E;
    const float mw = wave_max(sco);
    float p = __builtin_amdgcn_exp2f(sco - mw);
    const float lw = wave_sum(p) * 0.5f;
    if (lane == 0) *(LAS f2*)(l_ml + 2 * w) = f2{mw, lw};
    LDS_ORDER();
    l_f[F_ST + w] = 1u;
    PIN(p);
    SB();
    f2 oa[4], oc[4];
#pragma unroll
    for (int j = 0; j < 4; ++j) { oa[j] = f2{0.f, 0.f}; oc[j] = f2{0.f, 0.f}; }
#define PV_G(G) { const float pa = dpp<BCAST + 2 * (G)>(p), pb = dpp<BCAST + 8 + 2 * (G)>(p); \
    axpy(oa, pa, vr[3 * (G)]); axpy(oa, pb, vr[3 * (G) + 1]); axpy(oc, bit3 ? pb : pa, vr[3 * (G) + 2]); SB(); }
    PV_G(0) PV_G(1) PV_G(2)
#undef PV_G
    float* lo = l_o + w * 192;
    LAS f2* loa = (LAS f2*)(lo + 8 * (lane & 15) + 2 * (lane >> 4));
    float* loc = lo + 128 + 8 * c8 + rg;
    const float pa = dpp<BCAST + 6>(p), pb = dpp<BCAST + 14>(p); float pc = bit3 ? pb : pa;
    PIN(pc);
    axpy(oa, pa, vr[9]); SB();
    axpy(oa, pb, vr[10]);
    *loa = f2{swap16_add(swap32_add(oa[0].x, oa[2].x), swap32_add(oa[1].x, oa[3].x)), swap16_add(swap32_add(oa[0].y, oa[2].y), swap32_add(oa[1].y, oa[3].y))};
    SB();
    axpy(oc, pc, vr[11]);
    *loc = treduce8(oc, bit3);
    LDS_ORDER();
    l_f[F_DONE + w] = 1u;
  }
  if (w >= 8) return;
  if (w < 3) {
    if (w == 2) return;
    wait_flags(l_f + F_ST, 16, lane, 1);
    const f2 ml = *(const LAS f2*)(l_ml + 2 * c16);
    float M = ml.x;
    M = fmaxf(M, dpp<XOR1>(M)); M = fmaxf(M, dpp<XOR2>(M)); M = fmaxf(M, dpp<XOR7>(M)); M = fmaxf(M, dpp<ROR8>(M));
    const float sc = __builtin_amdgcn_exp2f(ml.x - M);
    if (w == 1) {
      float L = sc * ml.y;
      L += dpp<XOR1>(L); L += dpp<XOR2>(L); L += dpp<XOR7>(L); L += dpp<ROR8>(L);
      put_u(ru, lane == 0 ? WS_ST + b * 16 : OOB, tg, __float_as_uint(M), __float_as_uint(M) << 22, __float_as_uint(L), __float_as_uint(L) << 22);
      return;
    }
    const LAS f4* lo = (const LAS f4*)l_o + min(lane, 47);
    const int dst = lane < 48 ? WS_OP + ((h * 3 + r16) * 8 + s) * 256 + c16 * 16 : OOB;
    f4 o = {0.f, 0.f, 0.f, 0.f};
#define CMB(i) o += dpp<BCAST + (i)>(sc) * lo[(i) * 48];
    wait_flags(l_f + F_DONE, 12, lane, 0);
    CMB(0) CMB(1) CMB(2) CMB(3) CMB(4) CMB(5) CMB(6) CMB(7) CMB(8) CMB(9) CMB(10) CMB(11)
#undef CMB
    float s12 = dpp<BCAST + 12>(sc), s13 = dpp<BCAST + 13>(sc), s14 = dpp<BCAST + 14>(sc), s15 = dpp<BCAST + 15>(sc);
    PIN(o); PIN(s12); PIN(s13); PIN(s14); PIN(s15);
    wait_flags(l_f + F_DONE + 12, 4, lane, 0);
    o += s12 * lo[12 * 48]; o += s13 * lo[13 * 48]; o += s14 * lo[14 * 48]; o += s15 * lo[15 * 48];
    put_u(ru, dst, tg, o);
    l_f[F_PUB] = 1u;
    return;
  }
  if (b >= 240) return;
  const int j5 = w >= 4 ? w - 4 : 4;
  {
    float y;
    if (w == 3) {
      int go = WS_GP + lane * 16;
      int vz = 0; PIN(vz);
      v4u g[5];
#pragma nounroll
      for (unsigned spin = 0; spin < SPIN_CAP; ++spin) {
        PIN(go);
#pragma unroll
        for (int k = 0; k < 5; ++k) g[k] = get_u(ru, go, rgp * 5120 + k * 1024);
        bool ok = g_ok(get_g(ws + WS_IF + cc * 16 + vz), thi, tlo);
#pragma unroll
        for (int k = 0; k < 5; ++k) ok = ok & u_ok(g[k], tg);
        if (__all(ok)) break;
        __builtin_amdgcn_s_sleep(8);
      }
      f4 t = ((u_val(g[0]) + u_val(g[1])) + (u_val(g[2]) + u_val(g[3]))) + u_val(g[4]);
      const f4 z = {0.f, 0.f, 0.f, 0.f};
      const float gt = rbf(treduce4((r16 & 1) ? z : t)), up = rbf(treduce4((r16 & 1) ? t : z));
      y = rbf(rbf(gt / (1.0f + __expf(-gt))) * up);
    }
    const unsigned char* wb = (w >= 4 ? wo + (size_t)(256 * rgp + 64 * j5) * 1280 : w2 + (size_t)(64 * rgp) * 1280) + cc * 128;
    const __amdgpu_buffer_rsrc_t rw = __builtin_amdgcn_make_buffer_rsrc((void*)wb, 0, 63 * 1280 + 128, 0x00020000);
    __builtin_amdgcn_s_setprio(0);
    v4u wr[8];
#pragma unroll
    for (int i = 0; i < 8; ++i) wr[i] = ld_nt(rw, (32 * bit3 + r16) * 1280 + c8 * 16, i * 4 * 1280);
    SB();
    __builtin_amdgcn_s_setprio(1);
    f2 wf[32];
    if (w >= 4) {
      const int beta = 4 * rgp + j5, hd = beta / 3;
      int oo = WS_OP + lane * 16, so = WS_ST + r16 * 16;
      wait_flags(l_f + F_PUB, 1, lane, 1);
      v4u g[2], gs[2];
      PIN(so);
#pragma unroll
      for (int k = 0; k < 2; ++k) gs[k] = get_u(ru, so, hd * 128 + k * 64);
      SB();
#pragma unroll
      for (int i = 0; i < 8; ++i) PIN(wr[i]);
#pragma unroll
      for (int i = 0; i < 8; ++i)
#pragma unroll
        for (int k = 0; k < 4; ++k) { wf[4 * i + k] = f2{bf_lo(wr[i][k]), bf_hi(wr[i][k])}; PIN(wf[4 * i + k]); }
      SB();
#pragma nounroll
      for (unsigned spin = 0; spin < SPIN_CAP && !__all(u_ok(gs[0], tg) & u_ok(gs[1], tg)); ++spin) {
        __builtin_amdgcn_s_sleep(4);
        PIN(so);
#pragma unroll
        for (int k = 0; k < 2; ++k) gs[k] = get_u(ru, so, hd * 128 + k * 64);
      }
      PIN(oo);
#pragma unroll
      for (int k = 0; k < 2; ++k) g[k] = get_u(ru, oo, beta * 2048 + k * 1024);
      SB();
      const float M0 = __uint_as_float((gs[0].x << 10) | ((gs[0].y >> 12) & 1023u)), M1 = __uint_as_float((gs[1].x << 10) | ((gs[1].y >> 12) & 1023u));
      const float L0 = __uint_as_float((gs[0].z << 10) | ((gs[0].w >> 12) & 1023u)), L1 = __uint_as_float((gs[1].z << 10) | ((gs[1].w >> 12) & 1023u));
      const float M = wave_max(fmaxf(M0, M1));
      float c0 = __builtin_amdgcn_exp2f(M0 - M), c1 = __builtin_amdgcn_exp2f(M1 - M);
      float L = c0 * L0 + c1 * L1;
      L = swap16_add(L, L); L = swap32_add(L, L);
      L = 1.0f / L;
      c0 *= L; c1 *= L;
      PIN(c0); PIN(c1);
#pragma nounroll
      for (unsigned spin = 0; spin < SPIN_CAP; ++spin) {
        const bool k0 = u_ok(g[0], tg), k1 = u_ok(g[1], tg);
        if (__all(k0 & k1)) break;
        PIN(oo);
        const v4u n0 = get_u(ru, k0 ? OOB : oo, beta * 2048), n1 = get_u(ru, k1 ? OOB : oo, beta * 2048 + 1024);
        g[0] = k0 ? g[0] : n0; g[1] = k1 ? g[1] : n1;
      }
      y = rbf(treduce4(c0 * u_val(g[0]) + c1 * u_val(g[1])));
    } else {
#pragma unroll
      for (int i = 0; i < 8; ++i)
#pragma unroll
        for (int k = 0; k < 4; ++k) wf[4 * i + k] = f2{bf_lo(wr[i][k]), bf_hi(wr[i][k])};
    }
    f2 acc[4];
#pragma unroll
    for (int j = 0; j < 4; ++j) acc[j] = f2{0.f, 0.f};
#define GV(i) { const float y0 = dpp<BCAST + (i)>(y), y1 = dpp<BCAST + 8 + (i)>(y); const float yi = bit3 ? y1 : y0; \
    _Pragma("unroll") for (int k = 0; k < 4; ++k) acc[k] = __builtin_elementwise_fma(f2{yi, yi}, wf[4 * (i) + k], acc[k]); }
    GV(0) GV(1) GV(2) GV(3) GV(4) GV(5) GV(6) GV(7)
#undef GV
    l_gv[j5 * 64 + 8 * c8 + rg] = treduce8(acc, bit3);
    LDS_ORDER();
    unsigned old = 0;
    if (lane == 0) old = __hip_atomic_fetch_add((LAS unsigned*)(l_f + F_CNT), 1u, __ATOMIC_RELAXED, __HIP_MEMORY_SCOPE_WORKGROUP);
    LDS_ORDER();
    const float t = ((l_gv[lane] + l_gv[64 + lane]) + (l_gv[128 + lane] + l_gv[192 + lane])) + l_gv[256 + lane];
    const float xres = __uint_as_float((unsigned)((const LAS u16*)l_x)[cc * 64 + lane] << 16);
    if (__builtin_amdgcn_readfirstlane(old) != 4u) return;
    const int col = cc * 64 + lane;
    const bool bad = !(fabsf(t) < 32768.0f) || wave_max(fabsf(t)) < 0.000244140625f;
    if (__builtin_expect(__any(bad), 0)) {
      if (bad) put_f(ws + WS_FP + (size_t)rgp * 640 + col, thi, tlo, t);
      asm volatile("s_waitcnt vmcnt(0)" ::: "memory");
    }
    const float fl = floorf(t);
    const u64 fix = ((u64)(unsigned)(int)fl << 32) | (unsigned)fminf((t - fl) * 4294967296.0f, 4294967040.0f);
    const u64 add = bad ? 65ull : (fix << 11) | 1ull;
    const u64 tot = __hip_atomic_fetch_add(ws + WS_AC + (size_t)(cc * 8 + rg) * 32 + c8, add, RLX_AGENT) + add;
    if (((unsigned)tot & 63u) == 24u) {
      float r = (float)((double)((long long)tot >> 11) * 2.3283064365386963e-10);
      const unsigned nb = ((unsigned)tot >> 6) & 31u;
      if (__builtin_expect(nb != 0u, 0)) {
        float e = 0.f;
#pragma nounroll
        for (unsigned spin = 0; spin < SPIN_CAP; ++spin) {
          unsigned n = 0; e = 0.f;
#pragma nounroll
          for (int k = 0; k < 24; ++k) { const u64 v = get_g(ws + WS_FP + (size_t)k * 640 + col); if (g_ok(v, thi, tlo)) { e += g_val(v); ++n; } }
          if (n == nb) break;
        }
        r += e;
      }
      out[col] = __builtin_bit_cast(u16, (__bf16)(r + xres));
      if (col == 0) __hip_atomic_store(ws + WS_EPOCH, ((u64)e1hi << 32) | e1lo, RLX_AGENT);
    }
  }
}

extern "C" void kernel_launch(void* const* d_in, const int* in_sizes, int n_in, void* d_out, int out_size, void* d_ws, size_t ws_size,
                              hipStream_t stream) {
  (void)in_sizes; (void)n_in; (void)out_size; (void)ws_size;
  fused_decode<<<dim3(NWG), dim3(NTHR), 0, stream>>>((const u16*)d_in[0], (const unsigned char*)d_in[1], (const unsigned char*)d_in[2],
                                                      (const unsigned char*)d_in[3], (const unsigned char*)d_in[4], (const unsigned char*)d_in[6],
                                                      (const unsigned char*)d_in[7], (const unsigned char*)d_in[8], (u16*)d_out, (u64*)d_ws);
}
```

```cpp
#include <hip/hip_runtime.h>
#include <hip/hip_bf16.h>

typedef unsigned v4u __attribute__((ext_vector_type(4)));
typedef float f2 __attribute__((ext_vector_type(2)));
typedef float f4 __attribute__((ext_vector_type(4)));
typedef unsigned long long u64;
typedef unsigned short u16;
typedef __attribute__((ext_vector_type(2))) __bf16 bf16x2;
#define GAS __attribute__((address_space(1)))
#define LAS __attribute__((address_space(3)))
typedef GAS u64 gu64;
#define RLX_AGENT __ATOMIC_RELAXED, __HIP_MEMORY_SCOPE_AGENT

constexpr int DM = 640, SK = 32768, WIN = 4096;
constexpr int NWG = 256, NTHR = 1024;
constexpr int ROWB = 384;
constexpr int WS_QP = 0;
constexpr int WS_GP = WS_QP + NWG * 1024;
constexpr int WS_OP = WS_GP + 240 * 512;
constexpr int WS_ST = WS_OP + NWG * 768;
constexpr int WS_UEND = WS_ST + NWG * 16;
constexpr size_t WS_EPOCH = WS_UEND / 8;
constexpr size_t WS_FP = WS_EPOCH + 64;
constexpr size_t WS_AC = WS_FP + (size_t)24 * 640;
constexpr size_t WS_IF = WS_AC + (size_t)80 * 32;
constexpr unsigned SPIN_CAP = 1u << 18;
constexpr float LOG2E = 1.4426950408889634f;
constexpr int OOB = 0x7fffff00;

__device__ __forceinline__ float bf_lo(unsigned d) { return __uint_as_float(d << 16); }
__device__ __forceinline__ float bf_hi(unsigned d) { return __uint_as_float(d & 0xffff0000u); }
__device__ __forceinline__ float rbf(float x) { return (float)(__bf16)x; }
__device__ __forceinline__ float dot2(unsigned a, unsigned b, float acc) {
  return __builtin_amdgcn_fdot2_f32_bf16(__builtin_bit_cast(bf16x2, a), __builtin_bit_cast(bf16x2, b), acc, false);
}
template <int CTRL> __device__ __forceinline__ float dpp(float x) {
  return __builtin_bit_cast(float, __builtin_amdgcn_mov_dpp(__builtin_bit_cast(int, x), CTRL, 0xf, 0xf, true));
}
constexpr int XOR1 = 0xB1, XOR2 = 0x4E, XOR7 = 0x141, ROR8 = 0x128, BCAST = 0x150;
__device__ __forceinline__ float swap32_add(float a, float b) {
  auto r = __builtin_amdgcn_permlane32_swap(__float_as_uint(a), __float_as_uint(b), false, false);
  return __uint_as_float(r[0]) + __uint_as_float(r[1]);
}
__device__ __forceinline__ float swap16_add(float a, float b) {
  auto r = __builtin_amdgcn_permlane16_swap(__float_as_uint(a), __float_as_uint(b), false, false);
  return __uint_as_float(r[0]) + __uint_as_float(r[1]);
}
__device__ __forceinline__ float wave_sum(float x) {
  x += dpp<XOR1>(x); x += dpp<XOR2>(x); x += dpp<XOR7>(x); x += dpp<ROR8>(x);
  x = swap16_add(x, x); x = swap32_add(x, x);
  return x;
}
__device__ __forceinline__ float wave_max(float x) {
  x = fmaxf(x, dpp<XOR1>(x)); x = fmaxf(x, dpp<XOR2>(x)); x = fmaxf(x, dpp<XOR7>(x)); x = fmaxf(x, dpp<ROR8>(x));
  auto r = __builtin_amdgcn_permlane16_swap(__float_as_uint(x), __float_as_uint(x), false, false);
  x = fmaxf(__uint_as_float(r[0]), __uint_as_float(r[1]));
  auto t = __builtin_amdgcn_permlane32_swap(__float_as_uint(x), __float_as_uint(x), false, false);
  return fmaxf(__uint_as_float(t[0]), __uint_as_float(t[1]));
}
__device__ __forceinline__ float treduce8(const f2 (&a)[4], int bit3) {
  const float e = swap16_add(swap32_add(a[0].x, a[2].x), swap32_add(a[1].x, a[3].x));
  const float o = swap16_add(swap32_add(a[0].y, a[2].y), swap32_add(a[1].y, a[3].y));
  return (bit3 ? o : e) + dpp<ROR8>(bit3 ? e : o);
}
__device__ __forceinline__ void axpy(f2 (&acc)[4], float p, v4u v) {
#pragma unroll
  for (int k = 0; k < 4; ++k) acc[k] = __builtin_elementwise_fma(f2{p, p}, f2{bf_lo(v[k]), bf_hi(v[k])}, acc[k]);
}
__device__ __forceinline__ void put_bits(gu64* g, unsigned thi, unsigned tlo, unsigned bits) {
  const unsigned lo = __builtin_amdgcn_alignbit(tlo, bits, 8);
  __hip_atomic_store(g, ((u64)thi << 32) | lo, RLX_AGENT);
}
__device__ __forceinline__ void put_f(gu64* g, unsigned thi, unsigned tlo, float v) { put_bits(g, thi, tlo, __float_as_uint(v)); }
__device__ __forceinline__ u64 get_g(const gu64* g) { return __hip_atomic_load(const_cast<gu64*>(g), RLX_AGENT); }
__device__ __forceinline__ bool g_ok(u64 x, unsigned thi, unsigned tlo) { return ((unsigned)(x >> 32) == thi) & (((unsigned)x >> 24) == tlo); }
__device__ __forceinline__ float g_val(u64 x) { return __uint_as_float((unsigned)x << 8); }
__device__ __forceinline__ v4u unit_tags(unsigned e1lo) {
  const unsigned c = e1lo & 7u, s0 = ((e1lo >> 3) & 127u) << 3 | c;
  return v4u{s0, s0 ^ (8u | (((e1lo >> 10) & 63u) << 4)), ((e1lo >> 16) & 127u) << 3 | c, ((e1lo >> 23) & 127u) << 3 | c};
}
__device__ __forceinline__ void put_u(__amdgpu_buffer_rsrc_t r, int off, v4u tg, unsigned a, unsigned b, unsigned c, unsigned d) {
  const v4u u = {__builtin_amdgcn_alignbit(tg.x, a, 10), __builtin_amdgcn_alignbit(tg.y, b, 10), __builtin_amdgcn_alignbit(tg.z, c, 10), __builtin_amdgcn_alignbit(tg.w, d, 10)};
  __builtin_amdgcn_raw_buffer_store_b128(u, r, off, 0, 16);
}
__device__ __forceinline__ void put_u(__amdgpu_buffer_rsrc_t r, int off, v4u tg, f4 v) {
  put_u(r, off, tg, __float_as_uint(v.x) + 512u, __float_as_uint(v.y) + 512u, __float_as_uint(v.z) + 512u, __float_as_uint(v.w) + 512u);
}
__device__ __forceinline__ v4u get_u(__amdgpu_buffer_rsrc_t r, int off, int soff) {
  return __builtin_amdgcn_raw_buffer_load_b128(r, off, soff, 16);
}
__device__ __forceinline__ bool u_ok(v4u x, v4u tg) { return ((x.x >> 22) == tg.x) & ((x.y >> 22) == tg.y) & ((x.z >> 22) == tg.z) & ((x.w >> 22) == tg.w); }
__device__ __forceinline__ f4 u_val(v4u x) { return f4{__uint_as_float(x.x << 10), __uint_as_float(x.y << 10), __uint_as_float(x.z << 10), __uint_as_float(x.w << 10)}; }
__device__ __forceinline__ float treduce4(f4 v) { return swap16_add(swap32_add(v.x, v.z), swap32_add(v.y, v.w)); }
__device__ __forceinline__ u64 next_epoch(u64 e0) {
  const u64 e = ((u64)(unsigned)__builtin_amdgcn_readfirstlane((unsigned)(e0 >> 32)) << 32) | (unsigned)__builtin_amdgcn_readfirstlane((unsigned)e0);
  return e * 6364136223846793005ull + 1442695040888963407ull;
}
__device__ __forceinline__ v4u ld_nt(__amdgpu_buffer_rsrc_t r, int voff, int soff) { return __builtin_amdgcn_raw_buffer_load_b128(r, voff, soff, 19); }
#define SB() __builtin_amdgcn_sched_barrier(0)
#define PIN(x) asm volatile("" : "+v"(x))
#define LDS_ORDER() asm volatile("" ::: "memory")
#define LDS_FENCE() asm volatile("s_waitcnt lgkmcnt(0)" ::: "memory")

constexpr int F_DONE = 0, F_ST = 16, F_Q = 32, F_PUB = 36, F_X = 40, F_CNT = 41;

__device__ __forceinline__ void wait_flags(volatile LAS unsigned* f, int n, int lane, int sleep) {
  const int i = lane < n ? lane : 0;
  while (!__all(f[i] != 0u)) { if (sleep) __builtin_amdgcn_s_sleep(4); else __builtin_amdgcn_s_sleep(0); }
  asm volatile("" ::: "memory");
}

__global__ void __launch_bounds__(NTHR, 4)
fused_decode(const u16* __restrict__ xg, const unsigned char* __restrict__ Kc, const unsigned char* __restrict__ Vc,
             const unsigned char* __restrict__ wq, const unsigned char* __restrict__ wo, const unsigned char* __restrict__ w1,
             const unsigned char* __restrict__ w3, const unsigned char* __restrict__ w2, u16* __restrict__ out, u64* __restrict__ wsp) {
  __shared__ __attribute__((aligned(16))) float lds[16 * 192 + 96 + 32 + 320 + 648 + 64 + 4 + 320];
  float* l_o = lds;
  unsigned* l_q = (unsigned*)(lds + 3072);
  float* l_ml = lds + 3072 + 96;
  float* l_gv = lds + 3072 + 128;
  float* l_xn = lds + 3072 + 448;
  volatile LAS unsigned* l_f = (volatile LAS unsigned*)(lds + 3072 + 1096);
  volatile LAS unsigned* l_tag = (volatile LAS unsigned*)(lds + 3072 + 1160);
  unsigned* l_x = (unsigned*)(lds + 3072 + 1164);

  const int b = blockIdx.x, tid = threadIdx.x, lane = tid & 63, w = __builtin_amdgcn_readfirstlane(tid >> 6);
  const int h = b >> 3, s = b & 7;
  const int tile = s * 30 + h;
  const int rgp = tile / 10, cc = tile - rgp * 10;
  gu64* ws = (gu64*)wsp;
  const __amdgpu_buffer_rsrc_t ru = __builtin_amdgcn_make_buffer_rsrc((void*)wsp, 0, WS_UEND, 0x00020000);
  const size_t kvbase = ((size_t)h * SK + (SK - WIN) + s * 512 + w * 32) * ROWB;
  const __amdgpu_buffer_rsrc_t rk = __builtin_amdgcn_make_buffer_rsrc((void*)(Kc + kvbase), 0, 32 * ROWB, 0x00020000);
  const __amdgpu_buffer_rsrc_t rv = __builtin_amdgcn_make_buffer_rsrc((void*)(Vc + kvbase), 0, 32 * ROWB, 0x00020000);
  const int bit3 = (lane >> 3) & 1, rg = lane >> 3, c8 = lane & 7, r16 = lane >> 4, c16 = lane & 15;
  const int offA = (lane >> 4) * ROWB + (lane & 15) * 16;
  const int offC = ((lane >> 4) + 4 * bit3) * ROWB + 256 + c8 * 16;

  __builtin_amdgcn_s_setprio(1);
  if (w < 6) {
    if (w == 3) {
      int vz = 0; PIN(vz);
      const u64 e0 = get_g(ws + WS_EPOCH + vz);
      const v4u xa = *(const v4u*)(xg + lane * 8);
      const unsigned xb = *(const unsigned*)(xg + 512 + lane * 2);
      l_f[lane] = 0u;
      SB();
      asm volatile("s_waitcnt lgkmcnt(0)\n\ts_barrier" ::: "memory");
      SB();
      float xv[10];
#pragma unroll
      for (int i = 0; i < 4; ++i) { xv[2 * i] = bf_lo(xa[i]); xv[2 * i + 1] = bf_hi(xa[i]); }
      xv[8] = bf_lo(xb); xv[9] = bf_hi(xb);
      float sm = 0.f;
#pragma unroll
      for (int i = 0; i < 10; ++i) sm += xv[i];
      const float mean = wave_sum(sm) * (1.0f / DM);
      float sq = 0.f;
#pragma unroll
      for (int i = 0; i < 10; ++i) { const float a = xv[i] - mean; sq = fmaf(a, a, sq); }
      const float var = wave_sum(sq) * (1.0f / DM);
      const float mb = rbf(mean), rs = 1.0f / rbf(sqrtf(rbf(rbf(var) + 1e-5f)));
#pragma unroll
      for (int i = 0; i < 10; ++i) xv[i] = rbf(rbf(xv[i] - mb) * rs);
#pragma unroll
      for (int i = 0; i < 4; ++i) *(LAS f2*)(l_xn + lane * 8 + 2 * i) = f2{xv[2 * i], xv[2 * i + 1]};
      *(LAS f2*)(l_xn + 512 + lane * 2) = f2{xv[8], xv[9]};
      *(LAS v4u*)(l_x + lane * 4) = xa; l_x[256 + lane] = xb;
      const u64 e1 = next_epoch(e0);
      if (lane == 0) { l_tag[0] = (unsigned)e1; l_tag[1] = (unsigned)(e1 >> 32); }
      LDS_FENCE();
      l_f[F_X] = 1u;
    } else {
      const bool isq = w < 3;
      if (isq || b < 240) {
        const int nr = isq ? 10 : 8, row0 = isq ? 80 * s : 64 * cc, stride = isq ? 12288 : 3072;
        const unsigned char* wb = isq ? wq + h * 384 + w * 128 : (w == 4 ? w1 : w3) + rgp * 128;
        const __amdgpu_buffer_rsrc_t rw = __builtin_amdgcn_make_buffer_rsrc((void*)(wb + (size_t)row0 * stride), 0, (8 * nr - 1) * stride + 128, 0x00020000);
        const int woff = rg * nr * stride + c8 * 16, woff2 = isq ? woff : OOB;
        v4u wr[10];
#pragma unroll
        for (int i = 0; i < 10; ++i) { wr[i] = ld_nt(rw, i < 8 ? woff : woff2, i * stride); if (i == 1) { SB(); asm volatile("s_barrier" ::: "memory"); SB(); } }
        SB();
        wait_flags(l_f + F_X, 1, lane, 1);
        f2 xn[5];
#pragma unroll
        for (int i = 0; i < 5; ++i) xn[i] = *(const LAS f2*)(l_xn + row0 + nr * rg + 2 * i);
        if (!isq) xn[4] = f2{0.f, 0.f};
        const v4u tg = unit_tags(__builtin_amdgcn_readfirstlane(l_tag[0]));
        f2 acc[4];
#pragma unroll
        for (int j = 0; j < 4; ++j) acc[j] = f2{0.f, 0.f};
#pragma unroll
        for (int i = 0; i < 10; ++i) axpy(acc, xn[i >> 1][i & 1], wr[i]);
        f4 t = {swap32_add(acc[0].x, acc[2].x), swap32_add(acc[0].y, acc[2].y), swap32_add(acc[1].x, acc[3].x), swap32_add(acc[1].y, acc[3].y)};
#pragma unroll
        for (int j = 0; j < 4; ++j) { t[j] = swap16_add(t[j], t[j]); t[j] += dpp<ROR8>(t[j]); }
        const int dst = (isq ? WS_QP + b * 1024 + w * 256 : WS_GP + tile * 512 + (w - 4) * 256) + (2 * c8 + (lane >> 5)) * 16;
        put_u(ru, (lane & 24) ? OOB : dst, tg, t);
        SB();
      } else {
        asm volatile("s_barrier" ::: "memory");
        if (w == 4 && b < 250) {
          __hip_atomic_store(ws + WS_AC + (size_t)((b - 240) * 8 + rg) * 32 + c8, 0ull, RLX_AGENT);
          wait_flags(l_f + F_X, 1, lane, 1);
          const unsigned t0 = __builtin_amdgcn_readfirstlane(l_tag[0]), t1 = __builtin_amdgcn_readfirstlane(l_tag[1]);
          asm volatile("s_waitcnt vmcnt(0)" ::: "memory");
          if (lane == 0) put_bits(ws + WS_IF + (b - 240) * 16, (t0 >> 8) | (t1 << 24), t0 & 0xffu, 0u);
          SB();
        }
      }
    }
  } else {
    asm volatile("s_barrier" ::: "memory");
  }
  v4u kr[12], vr[12];
#pragma unroll
  for (int g = 0; g < 4; ++g) {
    kr[3 * g] = ld_nt(rk, offA, g * 3072);
    kr[3 * g + 1] = ld_nt(rk, offA, g * 3072 + 1536);
    kr[3 * g + 2] = ld_nt(rk, offC, g * 3072);
    SB();
  }
  if (w == 2) {
    const v4u tg = unit_tags(__builtin_amdgcn_readfirstlane(l_tag[0]));
    int qo = WS_QP + min(lane, 47) * 16;
    asm volatile("s_waitcnt vmcnt(0)" ::: "memory");
    v4u g[8];
#pragma nounroll
    for (unsigned spin = 0; spin < SPIN_CAP; ++spin) {
      bool ok = true;
      PIN(qo);
#pragma unroll
      for (int p = 0; p < 8; ++p) g[p] = get_u(ru, qo, (h * 8 + p) * 1024);
#pragma unroll
      for (int p = 0; p < 8; ++p) ok = ok & u_ok(g[p], tg);
      if (__all(ok)) break;
      __builtin_amdgcn_s_sleep(8);
    }
    f4 t = u_val(g[0]);
#pragma unroll
    for (int p = 1; p < 8; ++p) t += u_val(g[p]);
    const bf16x2 q01 = {(__bf16)t.x, (__bf16)t.y}, q23 = {(__bf16)t.z, (__bf16)t.w};
    if (lane < 48) { l_q[2 * lane] = __builtin_bit_cast(unsigned, q01); l_q[2 * lane + 1] = __builtin_bit_cast(unsigned, q23); }
    LDS_FENCE();
    l_f[F_Q] = 1u;
    SB();
  }
#pragma unroll
  for (int g = 0; g < 4; ++g) {
    vr[3 * g] = ld_nt(rv, offA, g * 3072);
    vr[3 * g + 1] = ld_nt(rv, offA, g * 3072 + 1536);
    vr[3 * g + 2] = ld_nt(rv, offC, g * 3072);
    SB();
  }
  wait_flags(l_f + F_Q, 1, lane, 1);
  const unsigned e1lo = __builtin_amdgcn_readfirstlane(l_tag[0]), e1hi = __builtin_amdgcn_readfirstlane(l_tag[1]);
  const unsigned thi = (e1lo >> 8) | (e1hi << 24), tlo = e1lo & 0xffu;
  const v4u tg = unit_tags(e1lo);
  {
    const v4u qa = *(const LAS v4u*)(l_q + (lane & 15) * 4);
    const v4u qc = *(const LAS v4u*)(l_q + 64 + c8 * 4);
    float xs[4];
#pragma unroll
    for (int g = 0; g < 4; ++g) {
      float sa = 0.f, sb = 0.f, sc = 0.f;
#pragma unroll
      for (int k = 0; k < 4; ++k) { sa = dot2(kr[3 * g][k], qa[k], sa); sb = dot2(kr[3 * g + 1][k], qa[k], sb); sc = dot2(kr[3 * g + 2][k], qc[k], sc); }
      const float keep = bit3 ? sb : sa, send = bit3 ? sa : sb;
      xs[g] = keep + sc + dpp<ROR8>(send);
      PIN(xs[g]);
    }
    float x2[2];
#pragma unroll
    for (int j = 0; j < 2; ++j) { const float keep = (lane & 4) ? xs[j + 2] : xs[j], send = (lane & 4) ? xs[j] : xs[j + 2]; x2[j] = keep + dpp<XOR7>(send); }
    float sco = ((lane & 2) ? x2[1] : x2[0]) + dpp<XOR2>((lane & 2) ? x2[0] : x2[1]);
    sco += dpp<XOR1>(sco);
    sco = rbf(rbf(sco) * 0.07216878364870322f) * LOG2E;
    const float mw = wave_max(sco);
    float p = __builtin_amdgcn_exp2f(sco - mw);
    const float lw = wave_sum(p) * 0.5f;
    if (lane == 0) *(LAS f2*)(l_ml + 2 * w) = f2{mw, lw};
    LDS_ORDER();
    l_f[F_ST + w] = 1u;
    PIN(p);
    SB();
    f2 oa[4], oc[4];
#pragma unroll
    for (int j = 0; j < 4; ++j) { oa[j] = f2{0.f, 0.f}; oc[j] = f2{0.f, 0.f}; }
#define PV_G(G) { const float pa = dpp<BCAST + 2 * (G)>(p), pb = dpp<BCAST + 8 + 2 * (G)>(p); \
    axpy(oa, pa, vr[3 * (G)]); axpy(oa, pb, vr[3 * (G) + 1]); axpy(oc, bit3 ? pb : pa, vr[3 * (G) + 2]); SB(); }
    PV_G(0) PV_G(1) PV_G(2)
#undef PV_G
    float* lo = l_o + w * 192;
    LAS f2* loa = (LAS f2*)(lo + 8 * (lane & 15) + 2 * (lane >> 4));
    float* loc = lo + 128 + 8 * c8 + rg;
    const float pa = dpp<BCAST + 6>(p), pb = dpp<BCAST + 14>(p); float pc = bit3 ? pb : pa;
    PIN(pc);
    axpy(oa, pa, vr[9]); SB();
    axpy(oa, pb, vr[10]);
    *loa = f2{swap16_add(swap32_add(oa[0].x, oa[2].x), swap32_add(oa[1].x, oa[3].x)), swap16_add(swap32_add(oa[0].y, oa[2].y), swap32_add(oa[1].y, oa[3].y))};
    SB();
    axpy(oc, pc, vr[11]);
    *loc = treduce8(oc, bit3);
    LDS_ORDER();
    l_f[F_DONE + w] = 1u;
  }
  if (w >= 8) return;
  if (w < 3) {
    if (w == 2) return;
    wait_flags(l_f + F_ST, 16, lane, 1);
    const f2 ml = *(const LAS f2*)(l_ml + 2 * c16);
    float M = ml.x;
    M = fmaxf(M, dpp<XOR1>(M)); M = fmaxf(M, dpp<XOR2>(M)); M = fmaxf(M, dpp<XOR7>(M)); M = fmaxf(M, dpp<ROR8>(M));
    const float sc = __builtin_amdgcn_exp2f(ml.x - M);
    if (w == 1) {
      float L = sc * ml.y;
      L += dpp<XOR1>(L); L += dpp<XOR2>(L); L += dpp<XOR7>(L); L += dpp<ROR8>(L);
      put_u(ru, lane == 0 ? WS_ST + b * 16 : OOB, tg, __float_as_uint(M), __float_as_uint(M) << 22, __float_as_uint(L), __float_as_uint(L) << 22);
      return;
    }
    const LAS f4* lo = (const LAS f4*)l_o + min(lane, 47);
    const int dst = lane < 48 ? WS_OP + ((h * 3 + r16) * 8 + s) * 256 + c16 * 16 : OOB;
    f4 o = {0.f, 0.f, 0.f, 0.f};
#define CMB(i) o += dpp<BCAST + (i)>(sc) * lo[(i) * 48];
    wait_flags(l_f + F_DONE, 12, lane, 0);
    CMB(0) CMB(1) CMB(2) CMB(3) CMB(4) CMB(5) CMB(6) CMB(7) CMB(8) CMB(9) CMB(10) CMB(11)
#undef CMB
    float s12 = dpp<BCAST + 12>(sc), s13 = dpp<BCAST + 13>(sc), s14 = dpp<BCAST + 14>(sc), s15 = dpp<BCAST + 15>(sc);
    PIN(o); PIN(s12); PIN(s13); PIN(s14); PIN(s15);
    wait_flags(l_f + F_DONE + 12, 4, lane, 0);
    o += s12 * lo[12 * 48]; o += s13 * lo[13 * 48]; o += s14 * lo[14 * 48]; o += s15 * lo[15 * 48];
    put_u(ru, dst, tg, o);
    l_f[F_PUB] = 1u;
    return;
  }
  if (b >= 240) return;
  const int j5 = w >= 4 ? w - 4 : 4;
  {
    float y;
    if (w == 3) {
      int go = WS_GP + lane * 16;
      int vz = 0; PIN(vz);
      v4u g[5];
#pragma nounroll
      for (unsigned spin = 0; spin < SPIN_CAP; ++spin) {
        PIN(go);
#pragma unroll
        for (int k = 0; k < 5; ++k) g[k] = get_u(ru, go, rgp * 5120 + k * 1024);
        bool ok = g_ok(get_g(ws + WS_IF + cc * 16 + vz), thi, tlo);
#pragma unroll
        for (int k = 0; k < 5; ++k) ok = ok & u_ok(g[k], tg);
        if (__all(ok)) break;
        __builtin_amdgcn_s_sleep(8);
      }
      f4 t = ((u_val(g[0]) + u_val(g[1])) + (u_val(g[2]) + u_val(g[3]))) + u_val(g[4]);
      const f4 z = {0.f, 0.f, 0.f, 0.f};
      const float gt = rbf(treduce4((r16 & 1) ? z : t)), up = rbf(treduce4((r16 & 1) ? t : z));
      y = rbf(rbf(gt / (1.0f + __expf(-gt))) * up);
    }
    const unsigned char* wb = (w >= 4 ? wo + (size_t)(256 * rgp + 64 * j5) * 1280 : w2 + (size_t)(64 * rgp) * 1280) + cc * 128;
    const __amdgpu_buffer_rsrc_t rw = __builtin_amdgcn_make_buffer_rsrc((void*)wb, 0, 63 * 1280 + 128, 0x00020000);
    __builtin_amdgcn_s_setprio(0);
    v4u wr[8];
#pragma unroll
    for (int i = 0; i < 8; ++i) wr[i] = ld_nt(rw, (32 * bit3 + r16) * 1280 + c8 * 16, i * 4 * 1280);
    SB();
    __builtin_amdgcn_s_setprio(1);
    f2 wf[32];
    if (w >= 4) {
      const int beta = 4 * rgp + j5, hd = beta / 3;
      int oo = WS_OP + lane * 16, so = WS_ST + r16 * 16;
      wait_flags(l_f + F_PUB, 1, lane, 1);
      v4u g[2], gs[2];
      PIN(so);
#pragma unroll
      for (int k = 0; k < 2; ++k) gs[k] = get_u(ru, so, hd * 128 + k * 64);
      SB();
#pragma unroll
      for (int i = 0; i < 8; ++i) PIN(wr[i]);
#pragma unroll
      for (int i = 0; i < 8; ++i)
#pragma unroll
        for (int k = 0; k < 4; ++k) { wf[4 * i + k] = f2{bf_lo(wr[i][k]), bf_hi(wr[i][k])}; PIN(wf[4 * i + k]); }
      SB();
#pragma nounroll
      for (unsigned spin = 0; spin < SPIN_CAP && !__all(u_ok(gs[0], tg) & u_ok(gs[1], tg)); ++spin) {
        __builtin_amdgcn_s_sleep(4);
        PIN(so);
#pragma unroll
        for (int k = 0; k < 2; ++k) gs[k] = get_u(ru, so, hd * 128 + k * 64);
      }
      PIN(oo);
#pragma unroll
      for (int k = 0; k < 2; ++k) g[k] = get_u(ru, oo, beta * 2048 + k * 1024);
      SB();
      const float M0 = __uint_as_float((gs[0].x << 10) | ((gs[0].y >> 12) & 1023u)), M1 = __uint_as_float((gs[1].x << 10) | ((gs[1].y >> 12) & 1023u));
      const float L0 = __uint_as_float((gs[0].z << 10) | ((gs[0].w >> 12) & 1023u)), L1 = __uint_as_float((gs[1].z << 10) | ((gs[1].w >> 12) & 1023u));
      const float M = wave_max(fmaxf(M0, M1));
      float c0 = __builtin_amdgcn_exp2f(M0 - M), c1 = __builtin_amdgcn_exp2f(M1 - M);
      float L = c0 * L0 + c1 * L1;
      L = swap16_add(L, L); L = swap32_add(L, L);
      L = 1.0f / L;
      c0 *= L; c1 *= L;
      PIN(c0); PIN(c1);
#pragma nounroll
      for (unsigned spin = 0; spin < SPIN_CAP; ++spin) {
        const bool k0 = u_ok(g[0], tg), k1 = u_ok(g[1], tg);
        if (__all(k0 & k1)) break;
        PIN(oo);
        const v4u n0 = get_u(ru, k0 ? OOB : oo, beta * 2048), n1 = get_u(ru, k1 ? OOB : oo, beta * 2048 + 1024);
        g[0] = k0 ? g[0] : n0; g[1] = k1 ? g[1] : n1;
      }
      y = rbf(treduce4(c0 * u_val(g[0]) + c1 * u_val(g[1])));
    } else {
#pragma unroll
      for (int i = 0; i < 8; ++i)
#pragma unroll
        for (int k = 0; k < 4; ++k) wf[4 * i + k] = f2{bf_lo(wr[i][k]), bf_hi(wr[i][k])};
    }
    f2 acc[4];
#pragma unroll
    for (int j = 0; j < 4; ++j) acc[j] = f2{0.f, 0.f};
#define GV(i) { const float y0 = dpp<BCAST + (i)>(y), y1 = dpp<BCAST + 8 + (i)>(y); const float yi = bit3 ? y1 : y0; \
    _Pragma("unroll") for (int k = 0; k < 4; ++k) acc[k] = __builtin_elementwise_fma(f2{yi, yi}, wf[4 * (i) + k], acc[k]); }
    GV(0) GV(1) GV(2) GV(3) GV(4) GV(5) GV(6) GV(7)
#undef GV
    l_gv[j5 * 64 + 8 * c8 + rg] = treduce8(acc, bit3);
    LDS_ORDER();
    unsigned old = 0;
    if (lane == 0) old = __hip_atomic_fetch_add((LAS unsigned*)(l_f + F_CNT), 1u, __ATOMIC_RELAXED, __HIP_MEMORY_SCOPE_WORKGROUP);
    LDS_ORDER();
    const float t = ((l_gv[lane] + l_gv[64 + lane]) + (l_gv[128 + lane] + l_gv[192 + lane])) + l_gv[256 + lane];
    const float xres = __uint_as_float((unsigned)((const LAS u16*)l_x)[cc * 64 + lane] << 16);
    if (__builtin_amdgcn_readfirstlane(old) != 4u) return;
    const int col = cc * 64 + lane;
    const float at = fabsf(t);
    const bool bad = !(at < 32768.0f) | !__any(at >= 0.000244140625f);
    if (__builtin_expect(__any(bad), 0)) {
      if (bad) put_f(ws + WS_FP + (size_t)rgp * 640 + col, thi, tlo, t);
      asm volatile("s_waitcnt vmcnt(0)" ::: "memory");
    }
    const float fl = floorf(at);
    const u64 mag = (((u64)(unsigned)fl << 32) | (unsigned)((at - fl) * 4294967296.0f)) << 11;
    const u64 add = bad ? 65ull : (t < 0.f ? 0ull - mag : mag) | 1ull;
    const u64 tot = __hip_atomic_fetch_add(ws + WS_AC + (size_t)(cc * 8 + rg) * 32 + c8, add, RLX_AGENT) + add;
    if (((unsigned)tot & 63u) == 24u) {
      float r = (float)((double)((long long)tot >> 11) * 2.3283064365386963e-10);
      const unsigned nb = ((unsigned)tot >> 6) & 31u;
      if (__builtin_expect(nb != 0u, 0)) {
        float e = 0.f;
#pragma nounroll
        for (unsigned spin = 0; spin < SPIN_CAP; ++spin) {
          unsigned n = 0; e = 0.f;
#pragma nounroll
          for (int k = 0; k < 24; ++k) { const u64 v = get_g(ws + WS_FP + (size_t)k * 640 + col); if (g_ok(v, thi, tlo)) { e += g_val(v); ++n; } }
          if (n == nb) break;
        }
        r += e;
      }
      out[col] = __builtin_bit_cast(u16, (__bf16)(r + xres));
      if (col == 0) __hip_atomic_store(ws + WS_EPOCH, ((u64)e1hi << 32) | e1lo, RLX_AGENT);
    }
  }
}

extern "C" void kernel_launch(void* const* d_in, const int* in_sizes, int n_in, void* d_out, int out_size, void* d_ws, size_t ws_size,
                              hipStream_t stream) {
  (void)in_sizes; (void)n_in; (void)out_size; (void)ws_size;
  fused_decode<<<dim3(NWG), dim3(NTHR), 0, stream>>>((const u16*)d_in[0], (const unsigned char*)d_in[1], (const unsigned char*)d_in[2],
                                                      (const unsigned char*)d_in[3], (const unsigned char*)d_in[4], (const unsigned char*)d_in[6],
                                                      (const unsigned char*)d_in[7], (const unsigned char*)d_in[8], (u16*)d_out, (u64*)d_ws);
}
```
